```python
import torch

def _softmax(x, axis=-1):
    return torch.softmax(x, dim=axis)
def _gelu(x):
    return torch.nn.functional.gelu(x, approximate="tanh")
def _silu(x):
    return torch.nn.functional.silu(x)
def _sigmoid(x):
    return torch.sigmoid(x)
def _layernorm(x, axis=-1, eps=1e-5):
    m = x.mean(dim=axis, keepdim=True)
    v = x.var(dim=axis, keepdim=True, unbiased=False)
    return (x - m) / torch.sqrt(v + eps)
def _scatter_add(tgt, idx, src, axis=0):
    return tgt.clone().index_add_(axis, idx, src)
def _segment_sum(src, seg, n):
    out = torch.zeros((n,) + src.shape[1:], dtype=src.dtype)
    return out.index_add_(0, seg, src)
def _pad(x, pw):
    flat = tuple(v for pair in reversed(pw) for v in pair)
    return torch.nn.functional.pad(x, flat)

kernel_name = 'recurrent_scan__spectral_fnet__moe_arch_mod__prompt'

def setup_inputs():

    _w = (lambda a: (lambda t: t.to(torch.bfloat16) if t.is_floating_point() else t)(torch.as_tensor(a)))
    def _A_setup():
        B, S, D = 1, 2048, 640
        ns = torch.arange(S)[:, None] * torch.arange(S)[None, :]
        nd = torch.arange(D)[:, None] * torch.arange(D)[None, :]
        Ws_re = torch.cos(-2.0 * torch.pi * ns / S).to(float)
        Ws_im = torch.sin(-2.0 * torch.pi * ns / S).to(float)
        Wd_re = torch.cos(-2.0 * torch.pi * nd / D).to(float)
        Wd_im = torch.sin(-2.0 * torch.pi * nd / D).to(float)
        return {
            "x": _w((lambda *s: torch.randn(*s, dtype=torch.bfloat16) * 0.1)(B, S, D)),
            "dft_seq_re": _w(Ws_re),
            "dft_seq_im": _w(Ws_im),
            "dft_hid_re": _w(Wd_re),
            "dft_hid_im": _w(Wd_im),
        }
    _A_in = _A_setup()

    def _B_setup():
        B, S, D, F, C = 1, 2048, 640, 1728, 2
        return {
            "x": _w((lambda *s: torch.randn(*s, dtype=torch.bfloat16) * 0.1)(B, S, D)),
            "w_r": _w((lambda *s: torch.randn(*s, dtype=torch.bfloat16) * 0.1)(D)),
            "w_gate": _w((lambda *s: torch.randn(*s, dtype=torch.bfloat16) * 0.1)(D, F)),
            "w_up": _w((lambda *s: torch.randn(*s, dtype=torch.bfloat16) * 0.1)(D, F)),
            "w_down": _w((lambda *s: torch.randn(*s, dtype=torch.bfloat16) * 0.1)(F, D)),
        }
    _B_in = _B_setup()

    return {
        "A_x": _A_in["x"],
        "A_dft_seq_re": _A_in["dft_seq_re"],
        "A_dft_seq_im": _A_in["dft_seq_im"],
        "A_dft_hid_re": _A_in["dft_hid_re"],
        "A_dft_hid_im": _A_in["dft_hid_im"],
        "B_x": _B_in["x"],
        "B_w_r": _B_in["w_r"],
        "B_w_gate": _B_in["w_gate"],
        "B_w_up": _B_in["w_up"],
        "B_w_down": _B_in["w_down"],
    }

def reference(A_x, A_dft_seq_re, A_dft_seq_im, A_dft_hid_re, A_dft_hid_im, B_x, B_w_r, B_w_gate, B_w_up, B_w_down):
    def _A_ref(x, dft_seq_re, dft_seq_im, dft_hid_re, dft_hid_im):
        a = torch.einsum("bsd,ed->bse", x, dft_hid_re)
        b = torch.einsum("bsd,ed->bse", x, dft_hid_im)
        y = torch.einsum("ts,bse->bte", dft_seq_re, a) - torch.einsum("ts,bse->bte", dft_seq_im, b)
        return y

    def _B_ref(x, w_r, w_gate, w_up, w_down):
        B, S, D = x.shape
        C = 2
        r = x @ w_r
        idx = torch.argsort(-r, axis=-1)[:, :C]
        idx_e = (lambda x, n, axis: torch.repeat_interleave(x, n, dim=axis))(idx.reshape(B, C, 1), D, axis=2)
        x_sel = (lambda x, i, axis: torch.take_along_dim(x, i, dim=axis))(x, idx_e, axis=1)
        g = _silu(x_sel @ w_gate)
        u = x_sel @ w_up
        y = (g * u) @ w_down
        r_sel = _sigmoid((lambda x, i, axis: torch.take_along_dim(x, i, dim=axis))(r, idx, axis=1))
        y = y * r_sel.reshape(B, C, 1)
        b_off = (torch.arange(B) * S).reshape(B, 1)
        flat_idx = (idx + b_off).reshape(B * C)
        zero = (x * 0.0).reshape(B * S, D)
        upd = _scatter_add(zero, flat_idx, y.reshape(B * C, D), axis=0)
        return x + upd.reshape(B, S, D)

    x = A_x
    xn = _layernorm(x, axis=-1)
    s = _A_ref(xn, A_dft_seq_re, A_dft_seq_im, A_dft_hid_re, A_dft_hid_im)
    g = _silu(_B_ref(xn, B_w_r, B_w_gate, B_w_up, B_w_down))
    return x + s * g
```

```cpp
#include <hip/hip_runtime.h>
#include <hip/hip_bf16.h>
#include <stdint.h>

typedef __attribute__((ext_vector_type(8))) short bf16x8;
typedef __attribute__((ext_vector_type(4))) float f32x4;
typedef unsigned short u16;

constexpr int S = 2048, D = 640, F = 1728;

__device__ __forceinline__ float bf2f(u16 v) { return __uint_as_float(((unsigned)v) << 16); }
__device__ __forceinline__ u16 f2bf(float f) { __hip_bfloat16 b = __float2bfloat16(f); return *(u16*)&b; }
__device__ __forceinline__ float rbf(float f) { return bf2f(f2bf(f)); }
__device__ __forceinline__ float wsum(float v) { for (int o = 32; o; o >>= 1) v += __shfl_xor(v, o); return v; }

__global__ void k_ln(const u16* x, const u16* wr, u16* xn, float* r) {
  int row = blockIdx.x * 4 + (threadIdx.x >> 6), lane = threadIdx.x & 63;
  float v[10]; float s = 0;
  for (int i = 0; i < 10; ++i) { v[i] = bf2f(x[row * D + i * 64 + lane]); s += v[i]; }
  float m = wsum(s) / D; float q = 0;
  for (int i = 0; i < 10; ++i) { float d = v[i] - m; q += d * d; }
  float var = rbf(wsum(q) / D); float mb = rbf(m);
  float sd = rbf(sqrtf(rbf(var + 1e-5f)));
  float dot = 0;
  for (int i = 0; i < 10; ++i) { float y = rbf(rbf(v[i] - mb) / sd); xn[row * D + i * 64 + lane] = f2bf(y); dot += y * bf2f(wr[i * 64 + lane]); }
  dot = wsum(dot);
  if (lane == 0) r[row] = rbf(dot);
}

__global__ void k_gemm1(const u16* xn, const u16* wre, const u16* wim, u16* abT) {
  int w = blockIdx.x * 4 + (threadIdx.x >> 6), lane = threadIdx.x & 63;
  int sb = w / 80, eb = w % 80;
  int e0 = eb * 16; bool im = e0 >= D; const u16* W = im ? wim : wre; if (im) e0 -= D;
  f32x4 acc = {0, 0, 0, 0};
  int fr = lane & 15, fq = lane >> 4;
  for (int k = 0; k < D; k += 32) {
    bf16x8 a = *(const bf16x8*)(xn + (sb * 16 + fr) * D + k + fq * 8);
    bf16x8 b = *(const bf16x8*)(W + (e0 + fr) * D + k + fq * 8);
    acc = __builtin_amdgcn_mfma_f32_16x16x32_bf16(a, b, acc, 0, 0, 0);
  }
  for (int j = 0; j < 4; ++j) {
    float v = im ? -acc[j] : acc[j];
    abT[(e0 + fr) * 4096 + (im ? 2048 : 0) + sb * 16 + fq * 4 + j] = f2bf(v);
  }
}

__device__ void top2(const float* r, int& i0, int& i1, float* sv, int* si) {
  int t = threadIdx.x;
  float b0 = -INFINITY, b1 = -INFINITY; int j0 = 0, j1 = 1;
  for (int i = t; i < S; i += 256) { float v = r[i]; if (v > b0) { b1 = b0; j1 = j0; b0 = v; j0 = i; } else if (v > b1) { b1 = v; j1 = i; } }
  sv[2 * t] = b0; sv[2 * t + 1] = b1; si[2 * t] = j0; si[2 * t + 1] = j1;
  __syncthreads();
  b0 = -INFINITY; b1 = -INFINITY; j0 = 0; j1 = 1;
  for (int i = 0; i < 512; ++i) { float v = sv[i]; if (v > b0) { b1 = b0; j1 = j0; b0 = v; j0 = si[i]; } else if (v > b1) { b1 = v; j1 = si[i]; } }
  i0 = j0; i1 = j1;
}

__global__ void k_ffn1(const u16* xn, const float* r, const u16* wg, const u16* wu, float* h, int* idx) {
  __shared__ float sv[512]; __shared__ int si[512];
  int i0, i1; top2(r, i0, i1, sv, si);
  if (blockIdx.x == 0 && threadIdx.x == 0) { idx[0] = i0; idx[1] = i1; }
  int f = blockIdx.x * 256 + threadIdx.x;
  if (f >= F) return;
  float g0 = 0, g1 = 0, u0 = 0, u1 = 0;
  for (int d = 0; d < D; ++d) {
    float a = bf2f(xn[i0 * D + d]), b = bf2f(xn[i1 * D + d]);
    float g = bf2f(wg[d * F + f]), u = bf2f(wu[d * F + f]);
    g0 += a * g; g1 += b * g; u0 += a * u; u1 += b * u;
  }
  g0 = rbf(g0); g1 = rbf(g1); u0 = rbf(u0); u1 = rbf(u1);
  g0 = rbf(g0 / (1.f + __expf(-g0))); g1 = rbf(g1 / (1.f + __expf(-g1)));
  h[f] = rbf(g0 * u0); h[F + f] = rbf(g1 * u1);
}

__global__ void k_ffn2(const float* h, const u16* wd, const float* r, const int* idx, float* upd) {
  int d = blockIdx.x * 64 + threadIdx.x;
  float y0 = 0, y1 = 0;
  for (int f = 0; f < F; ++f) { float w = bf2f(wd[f * D + d]); y0 += h[f] * w; y1 += h[F + f] * w; }
  float s0 = rbf(1.f / (1.f + __expf(-r[idx[0]]))), s1 = rbf(1.f / (1.f + __expf(-r[idx[1]])));
  upd[d] = rbf(rbf(y0) * s0); upd[D + d] = rbf(rbf(y1) * s1);
}

__global__ void k_gemm2(const u16* wsre, const u16* wsim, const u16* abT, const u16* x, const u16* xn, const float* upd, const int* idx, u16* out) {
  int w = blockIdx.x * 4 + (threadIdx.x >> 6), lane = threadIdx.x & 63;
  int tb = w / 40, eb = w % 40;
  f32x4 acc = {0, 0, 0, 0};
  int fr = lane & 15, fq = lane >> 4;
  for (int k = 0; k < 4096; k += 32) {
    const u16* A = k < 2048 ? wsre + k : wsim + (k - 2048);
    bf16x8 a = *(const bf16x8*)(A + (tb * 16 + fr) * S + fq * 8);
    bf16x8 b = *(const bf16x8*)(abT + (eb * 16 + fr) * 4096 + k + fq * 8);
    acc = __builtin_amdgcn_mfma_f32_16x16x32_bf16(a, b, acc, 0, 0, 0);
  }
  int i0 = idx[0], i1 = idx[1];
  for (int j = 0; j < 4; ++j) {
    int t = tb * 16 + fq * 4 + j, e = eb * 16 + fr;
    float s = rbf(acc[j]);
    float B = bf2f(xn[t * D + e]);
    if (t == i0) B = rbf(B + upd[e]);
    if (t == i1) B = rbf(B + upd[D + e]);
    float g = rbf(B / (1.f + __expf(-B)));
    out[t * D + e] = f2bf(bf2f(x[t * D + e]) + rbf(s * g));
  }
}

extern "C" void kernel_launch(void* const* d_in, const int* in_sizes, int n_in,
                              void* d_out, int out_size, void* d_ws, size_t ws_size,
                              hipStream_t stream) {
  const u16* x = (const u16*)d_in[0];
  const u16* wsre = (const u16*)d_in[1];
  const u16* wsim = (const u16*)d_in[2];
  const u16* wdre = (const u16*)d_in[3];
  const u16* wdim = (const u16*)d_in[4];
  const u16* wr = (const u16*)d_in[6];
  const u16* wg = (const u16*)d_in[7];
  const u16* wu = (const u16*)d_in[8];
  const u16* wdn = (const u16*)d_in[9];
  char* ws = (char*)d_ws;
  u16* xn = (u16*)ws;
  u16* abT = (u16*)(ws + (4 << 20));
  float* r = (float*)(ws + (12 << 20));
  float* h = (float*)(ws + (12 << 20) + 16384);
  float* upd = (float*)(ws + (12 << 20) + 32768);
  int* idx = (int*)(ws + (12 << 20) + 49152);
  k_ln<<<S / 4, 256, 0, stream>>>(x, wr, xn, r);
  k_gemm1<<<128 * 80 / 4, 256, 0, stream>>>(xn, wdre, wdim, abT);
  k_ffn1<<<7, 256, 0, stream>>>(xn, r, wg, wu, h, idx);
  k_ffn2<<<10, 64, 0, stream>>>(h, wdn, r, idx, upd);
  k_gemm2<<<128 * 40 / 4, 256, 0, stream>>>(wsre, wsim, abT, x, xn, upd, idx, (u16*)d_out);
}
```

```cpp
#include <hip/hip_runtime.h>
#include <hip/hip_bf16.h>
#include <stdint.h>

typedef __attribute__((ext_vector_type(8))) short bf16x8;
typedef __attribute__((ext_vector_type(4))) float f32x4;
typedef unsigned short u16;

constexpr int S = 2048, D = 640, F = 1728;

__device__ __forceinline__ float bf2f(u16 v) { return __uint_as_float(((unsigned)v) << 16); }
__device__ __forceinline__ u16 f2bf(float f) { __hip_bfloat16 b = __float2bfloat16(f); return *(u16*)&b; }
__device__ __forceinline__ float rbf(float f) { return bf2f(f2bf(f)); }
__device__ __forceinline__ float wsum(float v) { for (int o = 32; o; o >>= 1) v += __shfl_xor(v, o); return v; }

__global__ void k_ln(const u16* x, const u16* wr, u16* xn, float* r) {
  int row = blockIdx.x * 4 + (threadIdx.x >> 6), lane = threadIdx.x & 63;
  float v[10]; float s = 0;
  for (int i = 0; i < 10; ++i) { v[i] = bf2f(x[row * D + i * 64 + lane]); s += v[i]; }
  float m = wsum(s) / D; float q = 0;
  for (int i = 0; i < 10; ++i) { float d = v[i] - m; q += d * d; }
  float var = rbf(wsum(q) / D); float mb = rbf(m);
  float sd = rbf(sqrtf(rbf(var + 1e-5f)));
  float dot = 0;
  for (int i = 0; i < 10; ++i) { float y = rbf(rbf(v[i] - mb) / sd); xn[row * D + i * 64 + lane] = f2bf(y); dot += y * bf2f(wr[i * 64 + lane]); }
  dot = wsum(dot);
  if (lane == 0) r[row] = rbf(dot);
}

__global__ void k_gemm1(const u16* xn, const u16* wre, const u16* wim, u16* abT) {
  int w = blockIdx.x * 4 + (threadIdx.x >> 6), lane = threadIdx.x & 63;
  int sb = w / 80, eb = w % 80;
  int e0 = eb * 16; bool im = e0 >= D; const u16* W = im ? wim : wre; if (im) e0 -= D;
  f32x4 acc = {0, 0, 0, 0};
  int fr = lane & 15, fq = lane >> 4;
  for (int k = 0; k < D; k += 32) {
    bf16x8 a = *(const bf16x8*)(xn + (sb * 16 + fr) * D + k + fq * 8);
    bf16x8 b = *(const bf16x8*)(W + (e0 + fr) * D + k + fq * 8);
    acc = __builtin_amdgcn_mfma_f32_16x16x32_bf16(a, b, acc, 0, 0, 0);
  }
  for (int j = 0; j < 4; ++j) {
    float v = im ? -acc[j] : acc[j];
    abT[(e0 + fr) * 4096 + (im ? 2048 : 0) + sb * 16 + fq * 4 + j] = f2bf(v);
  }
}

__device__ void top2(const float* r, int& i0, int& i1, float* sv, int* si) {
  int t = threadIdx.x;
  float b0 = -INFINITY, b1 = -INFINITY; int j0 = 0, j1 = 1;
  for (int i = t; i < S; i += 256) { float v = r[i]; if (v > b0) { b1 = b0; j1 = j0; b0 = v; j0 = i; } else if (v > b1) { b1 = v; j1 = i; } }
  sv[2 * t] = b0; sv[2 * t + 1] = b1; si[2 * t] = j0; si[2 * t + 1] = j1;
  __syncthreads();
  b0 = -INFINITY; b1 = -INFINITY; j0 = 0; j1 = 1;
  for (int i = 0; i < 512; ++i) { float v = sv[i]; if (v > b0) { b1 = b0; j1 = j0; b0 = v; j0 = si[i]; } else if (v > b1) { b1 = v; j1 = si[i]; } }
  i0 = j0; i1 = j1;
}

__global__ void k_ffn1(const u16* xn, const float* r, const u16* wg, const u16* wu, float* h, int* idx) {
  __shared__ float sv[512]; __shared__ int si[512];
  int i0, i1; top2(r, i0, i1, sv, si);
  if (blockIdx.x == 0 && threadIdx.x == 0) { idx[0] = i0; idx[1] = i1; }
  int f = blockIdx.x * 256 + threadIdx.x;
  if (f >= F) return;
  float g0 = 0, g1 = 0, u0 = 0, u1 = 0;
  for (int d = 0; d < D; ++d) {
    float a = bf2f(xn[i0 * D + d]), b = bf2f(xn[i1 * D + d]);
    float g = bf2f(wg[d * F + f]), u = bf2f(wu[d * F + f]);
    g0 += a * g; g1 += b * g; u0 += a * u; u1 += b * u;
  }
  g0 = rbf(g0); g1 = rbf(g1); u0 = rbf(u0); u1 = rbf(u1);
  g0 = rbf(g0 / (1.f + __expf(-g0))); g1 = rbf(g1 / (1.f + __expf(-g1)));
  h[f] = rbf(g0 * u0); h[F + f] = rbf(g1 * u1);
}

__global__ void k_ffn2(const float* h, const u16* wd, const float* r, const int* idx, float* upd) {
  int d = blockIdx.x * 64 + threadIdx.x;
  float y0 = 0, y1 = 0;
  for (int f = 0; f < F; ++f) { float w = bf2f(wd[f * D + d]); y0 += h[f] * w; y1 += h[F + f] * w; }
  float s0 = rbf(1.f / (1.f + __expf(-r[idx[0]]))), s1 = rbf(1.f / (1.f + __expf(-r[idx[1]])));
  upd[d] = rbf(rbf(y0) * s0); upd[D + d] = rbf(rbf(y1) * s1);
}

__global__ void k_gemm2(const u16* wsre, const u16* wsim, const u16* abT, const u16* x, const u16* xn, const float* upd, const int* idx, u16* out) {
  int w = blockIdx.x * 4 + (threadIdx.x >> 6), lane = threadIdx.x & 63;
  int tb = w / 40, eb = w % 40;
  f32x4 acc = {0, 0, 0, 0};
  int fr = lane & 15, fq = lane >> 4;
  for (int k = 0; k < 4096; k += 32) {
    const u16* A = k < 2048 ? wsre + k : wsim + (k - 2048);
    bf16x8 a = *(const bf16x8*)(A + (tb * 16 + fr) * S + fq * 8);
    bf16x8 b = *(const bf16x8*)(abT + (eb * 16 + fr) * 4096 + k + fq * 8);
    acc = __builtin_amdgcn_mfma_f32_16x16x32_bf16(a, b, acc, 0, 0, 0);
  }
  int i0 = idx[0], i1 = idx[1];
  for (int j = 0; j < 4; ++j) {
    int t = tb * 16 + fq * 4 + j, e = eb * 16 + fr;
    float s = rbf(acc[j]);
    float B = bf2f(xn[t * D + e]);
    if (t == i0) B = rbf(B + upd[e]);
    if (t == i1) B = rbf(B + upd[D + e]);
    float g = rbf(B / (1.f + __expf(-B)));
    out[t * D + e] = f2bf(bf2f(x[t * D + e]) + rbf(s * g));
  }
}


__global__ void k_probe(const char* p, unsigned stride, int n, unsigned* sink) {
  unsigned acc = 0; const char* a = p;
  for (int i = 0; i < n; ++i) {
    unsigned v;
    asm volatile("global_load_dword %0, %1, off\n s_waitcnt vmcnt(0)" : "=v"(v) : "v"(a) : "memory");
    acc ^= v; a += stride;
  }
  *sink = acc;
}

extern "C" void kernel_launch(void* const* d_in, const int* in_sizes, int n_in,
                              void* d_out, int out_size, void* d_ws, size_t ws_size,
                              hipStream_t stream) {
  const u16* x = (const u16*)d_in[0];
  const u16* wsre = (const u16*)d_in[1];
  const u16* wsim = (const u16*)d_in[2];
  const u16* wdre = (const u16*)d_in[3];
  const u16* wdim = (const u16*)d_in[4];
  const u16* wr = (const u16*)d_in[6];
  const u16* wg = (const u16*)d_in[7];
  const u16* wu = (const u16*)d_in[8];
  const u16* wdn = (const u16*)d_in[9];
  char* ws = (char*)d_ws;
  u16* xn = (u16*)ws;
  u16* abT = (u16*)(ws + (4 << 20));
  float* r = (float*)(ws + (12 << 20));
  float* h = (float*)(ws + (12 << 20) + 16384);
  float* upd = (float*)(ws + (12 << 20) + 32768);
  int* idx = (int*)(ws + (12 << 20) + 49152);
  k_probe<<<1,64,0,stream>>>((const char*)d_in[9], 512, 4000, (unsigned*)(ws + (13<<20)));
  k_ln<<<S / 4, 256, 0, stream>>>(x, wr, xn, r);
  k_gemm1<<<128 * 80 / 4, 256, 0, stream>>>(xn, wdre, wdim, abT);
  k_ffn1<<<7, 256, 0, stream>>>(xn, r, wg, wu, h, idx);
  k_ffn2<<<10, 64, 0, stream>>>(h, wdn, r, idx, upd);
  k_gemm2<<<128 * 40 / 4, 256, 0, stream>>>(wsre, wsim, abT, x, xn, upd, idx, (u16*)d_out);
}
```

```cpp
#include <hip/hip_runtime.h>
#include <hip/hip_bf16.h>
#include <stdint.h>

typedef __attribute__((ext_vector_type(8))) short bf16x8;
typedef __attribute__((ext_vector_type(4))) float f32x4;
typedef unsigned short u16;

constexpr int S = 2048, D = 640, F = 1728;

__device__ __forceinline__ float bf2f(u16 v) { return __uint_as_float(((unsigned)v) << 16); }
__device__ __forceinline__ u16 f2bf(float f) { __hip_bfloat16 b = __float2bfloat16(f); return *(u16*)&b; }
__device__ __forceinline__ float rbf(float f) { return bf2f(f2bf(f)); }
__device__ __forceinline__ float wsum(float v) { for (int o = 32; o; o >>= 1) v += __shfl_xor(v, o); return v; }

__global__ void k_ln(const u16* x, const u16* wr, u16* xn, float* r) {
  int row = blockIdx.x * 4 + (threadIdx.x >> 6), lane = threadIdx.x & 63;
  float v[10]; float s = 0;
  for (int i = 0; i < 10; ++i) { v[i] = bf2f(x[row * D + i * 64 + lane]); s += v[i]; }
  float m = wsum(s) / D; float q = 0;
  for (int i = 0; i < 10; ++i) { float d = v[i] - m; q += d * d; }
  float var = rbf(wsum(q) / D); float mb = rbf(m);
  float sd = rbf(sqrtf(rbf(var + 1e-5f)));
  float dot = 0;
  for (int i = 0; i < 10; ++i) { float y = rbf(rbf(v[i] - mb) / sd); xn[row * D + i * 64 + lane] = f2bf(y); dot += y * bf2f(wr[i * 64 + lane]); }
  dot = wsum(dot);
  if (lane == 0) r[row] = rbf(dot);
}

__global__ void k_gemm1(const u16* xn, const u16* wre, const u16* wim, u16* abT) {
  int w = blockIdx.x * 4 + (threadIdx.x >> 6), lane = threadIdx.x & 63;
  int sb = w / 80, eb = w % 80;
  int e0 = eb * 16; bool im = e0 >= D; const u16* W = im ? wim : wre; if (im) e0 -= D;
  f32x4 acc = {0, 0, 0, 0};
  int fr = lane & 15, fq = lane >> 4;
  for (int k = 0; k < D; k += 32) {
    bf16x8 a = *(const bf16x8*)(xn + (sb * 16 + fr) * D + k + fq * 8);
    bf16x8 b = *(const bf16x8*)(W + (e0 + fr) * D + k + fq * 8);
    acc = __builtin_amdgcn_mfma_f32_16x16x32_bf16(a, b, acc, 0, 0, 0);
  }
  for (int j = 0; j < 4; ++j) {
    float v = im ? -acc[j] : acc[j];
    abT[(e0 + fr) * 4096 + (im ? 2048 : 0) + sb * 16 + fq * 4 + j] = f2bf(v);
  }
}

__device__ void top2(const float* r, int& i0, int& i1, float* sv, int* si) {
  int t = threadIdx.x;
  float b0 = -INFINITY, b1 = -INFINITY; int j0 = 0, j1 = 1;
  for (int i = t; i < S; i += 256) { float v = r[i]; if (v > b0) { b1 = b0; j1 = j0; b0 = v; j0 = i; } else if (v > b1) { b1 = v; j1 = i; } }
  sv[2 * t] = b0; sv[2 * t + 1] = b1; si[2 * t] = j0; si[2 * t + 1] = j1;
  __syncthreads();
  b0 = -INFINITY; b1 = -INFINITY; j0 = 0; j1 = 1;
  for (int i = 0; i < 512; ++i) { float v = sv[i]; if (v > b0) { b1 = b0; j1 = j0; b0 = v; j0 = si[i]; } else if (v > b1) { b1 = v; j1 = si[i]; } }
  i0 = j0; i1 = j1;
}

__global__ void k_ffn1(const u16* xn, const float* r, const u16* wg, const u16* wu, float* h, int* idx) {
  __shared__ float sv[512]; __shared__ int si[512];
  int i0, i1; top2(r, i0, i1, sv, si);
  if (blockIdx.x == 0 && threadIdx.x == 0) { idx[0] = i0; idx[1] = i1; }
  int f = blockIdx.x * 256 + threadIdx.x;
  if (f >= F) return;
  float g0 = 0, g1 = 0, u0 = 0, u1 = 0;
  for (int d = 0; d < D; ++d) {
    float a = bf2f(xn[i0 * D + d]), b = bf2f(xn[i1 * D + d]);
    float g = bf2f(wg[d * F + f]), u = bf2f(wu[d * F + f]);
    g0 += a * g; g1 += b * g; u0 += a * u; u1 += b * u;
  }
  g0 = rbf(g0); g1 = rbf(g1); u0 = rbf(u0); u1 = rbf(u1);
  g0 = rbf(g0 / (1.f + __expf(-g0))); g1 = rbf(g1 / (1.f + __expf(-g1)));
  h[f] = rbf(g0 * u0); h[F + f] = rbf(g1 * u1);
}

__global__ void k_ffn2(const float* h, const u16* wd, const float* r, const int* idx, float* upd) {
  int d = blockIdx.x * 64 + threadIdx.x;
  float y0 = 0, y1 = 0;
  for (int f = 0; f < F; ++f) { float w = bf2f(wd[f * D + d]); y0 += h[f] * w; y1 += h[F + f] * w; }
  float s0 = rbf(1.f / (1.f + __expf(-r[idx[0]]))), s1 = rbf(1.f / (1.f + __expf(-r[idx[1]])));
  upd[d] = rbf(rbf(y0) * s0); upd[D + d] = rbf(rbf(y1) * s1);
}

__global__ void k_gemm2(const u16* wsre, const u16* wsim, const u16* abT, const u16* x, const u16* xn, const float* upd, const int* idx, u16* out) {
  int w = blockIdx.x * 4 + (threadIdx.x >> 6), lane = threadIdx.x & 63;
  int tb = w / 40, eb = w % 40;
  f32x4 acc = {0, 0, 0, 0};
  int fr = lane & 15, fq = lane >> 4;
  for (int k = 0; k < 4096; k += 32) {
    const u16* A = k < 2048 ? wsre + k : wsim + (k - 2048);
    bf16x8 a = *(const bf16x8*)(A + (tb * 16 + fr) * S + fq * 8);
    bf16x8 b = *(const bf16x8*)(abT + (eb * 16 + fr) * 4096 + k + fq * 8);
    acc = __builtin_amdgcn_mfma_f32_16x16x32_bf16(a, b, acc, 0, 0, 0);
  }
  int i0 = idx[0], i1 = idx[1];
  for (int j = 0; j < 4; ++j) {
    int t = tb * 16 + fq * 4 + j, e = eb * 16 + fr;
    float s = rbf(acc[j]);
    float B = bf2f(xn[t * D + e]);
    if (t == i0) B = rbf(B + upd[e]);
    if (t == i1) B = rbf(B + upd[D + e]);
    float g = rbf(B / (1.f + __expf(-B)));
    out[t * D + e] = f2bf(bf2f(x[t * D + e]) + rbf(s * g));
  }
}


__global__ void k_probe(unsigned* sink) {
  asm volatile("s_branch LJ0_%=\n"
".p2align 7\nLJ0_%=:\n s_branch LJ1_%=\n"
".p2align 7\nLJ1_%=:\n s_branch LJ2_%=\n"
".p2align 7\nLJ2_%=:\n s_branch LJ3_%=\n"
".p2align 7\nLJ3_%=:\n s_branch LJ4_%=\n"
".p2align 7\nLJ4_%=:\n s_branch LJ5_%=\n"
".p2align 7\nLJ5_%=:\n s_branch LJ6_%=\n"
".p2align 7\nLJ6_%=:\n s_branch LJ7_%=\n"
".p2align 7\nLJ7_%=:\n s_branch LJ8_%=\n"
".p2align 7\nLJ8_%=:\n s_branch LJ9_%=\n"
".p2align 7\nLJ9_%=:\n s_branch LJ10_%=\n"
".p2align 7\nLJ10_%=:\n s_branch LJ11_%=\n"
".p2align 7\nLJ11_%=:\n s_branch LJ12_%=\n"
".p2align 7\nLJ12_%=:\n s_branch LJ13_%=\n"
".p2align 7\nLJ13_%=:\n s_branch LJ14_%=\n"
".p2align 7\nLJ14_%=:\n s_branch LJ15_%=\n"
".p2align 7\nLJ15_%=:\n s_branch LJ16_%=\n"
".p2align 7\nLJ16_%=:\n s_branch LJ17_%=\n"
".p2align 7\nLJ17_%=:\n s_branch LJ18_%=\n"
".p2align 7\nLJ18_%=:\n s_branch LJ19_%=\n"
".p2align 7\nLJ19_%=:\n s_branch LJ20_%=\n"
".p2align 7\nLJ20_%=:\n s_branch LJ21_%=\n"
".p2align 7\nLJ21_%=:\n s_branch LJ22_%=\n"
".p2align 7\nLJ22_%=:\n s_branch LJ23_%=\n"
".p2align 7\nLJ23_%=:\n s_branch LJ24_%=\n"
".p2align 7\nLJ24_%=:\n s_branch LJ25_%=\n"
".p2align 7\nLJ25_%=:\n s_branch LJ26_%=\n"
".p2align 7\nLJ26_%=:\n s_branch LJ27_%=\n"
".p2align 7\nLJ27_%=:\n s_branch LJ28_%=\n"
".p2align 7\nLJ28_%=:\n s_branch LJ29_%=\n"
".p2align 7\nLJ29_%=:\n s_branch LJ30_%=\n"
".p2align 7\nLJ30_%=:\n s_branch LJ31_%=\n"
".p2align 7\nLJ31_%=:\n s_branch LJ32_%=\n"
".p2align 7\nLJ32_%=:\n s_branch LJ33_%=\n"
".p2align 7\nLJ33_%=:\n s_branch LJ34_%=\n"
".p2align 7\nLJ34_%=:\n s_branch LJ35_%=\n"
".p2align 7\nLJ35_%=:\n s_branch LJ36_%=\n"
".p2align 7\nLJ36_%=:\n s_branch LJ37_%=\n"
".p2align 7\nLJ37_%=:\n s_branch LJ38_%=\n"
".p2align 7\nLJ38_%=:\n s_branch LJ39_%=\n"
".p2align 7\nLJ39_%=:\n s_branch LJ40_%=\n"
".p2align 7\nLJ40_%=:\n s_branch LJ41_%=\n"
".p2align 7\nLJ41_%=:\n s_branch LJ42_%=\n"
".p2align 7\nLJ42_%=:\n s_branch LJ43_%=\n"
".p2align 7\nLJ43_%=:\n s_branch LJ44_%=\n"
".p2align 7\nLJ44_%=:\n s_branch LJ45_%=\n"
".p2align 7\nLJ45_%=:\n s_branch LJ46_%=\n"
".p2align 7\nLJ46_%=:\n s_branch LJ47_%=\n"
".p2align 7\nLJ47_%=:\n s_branch LJ48_%=\n"
".p2align 7\nLJ48_%=:\n s_branch LJ49_%=\n"
".p2align 7\nLJ49_%=:\n s_branch LJ50_%=\n"
".p2align 7\nLJ50_%=:\n s_branch LJ51_%=\n"
".p2align 7\nLJ51_%=:\n s_branch LJ52_%=\n"
".p2align 7\nLJ52_%=:\n s_branch LJ53_%=\n"
".p2align 7\nLJ53_%=:\n s_branch LJ54_%=\n"
".p2align 7\nLJ54_%=:\n s_branch LJ55_%=\n"
".p2align 7\nLJ55_%=:\n s_branch LJ56_%=\n"
".p2align 7\nLJ56_%=:\n s_branch LJ57_%=\n"
".p2align 7\nLJ57_%=:\n s_branch LJ58_%=\n"
".p2align 7\nLJ58_%=:\n s_branch LJ59_%=\n"
".p2align 7\nLJ59_%=:\n s_branch LJ60_%=\n"
".p2align 7\nLJ60_%=:\n s_branch LJ61_%=\n"
".p2align 7\nLJ61_%=:\n s_branch LJ62_%=\n"
".p2align 7\nLJ62_%=:\n s_branch LJ63_%=\n"
".p2align 7\nLJ63_%=:\n s_branch LJ64_%=\n"
".p2align 7\nLJ64_%=:\n s_branch LJ65_%=\n"
".p2align 7\nLJ65_%=:\n s_branch LJ66_%=\n"
".p2align 7\nLJ66_%=:\n s_branch LJ67_%=\n"
".p2align 7\nLJ67_%=:\n s_branch LJ68_%=\n"
".p2align 7\nLJ68_%=:\n s_branch LJ69_%=\n"
".p2align 7\nLJ69_%=:\n s_branch LJ70_%=\n"
".p2align 7\nLJ70_%=:\n s_branch LJ71_%=\n"
".p2align 7\nLJ71_%=:\n s_branch LJ72_%=\n"
".p2align 7\nLJ72_%=:\n s_branch LJ73_%=\n"
".p2align 7\nLJ73_%=:\n s_branch LJ74_%=\n"
".p2align 7\nLJ74_%=:\n s_branch LJ75_%=\n"
".p2align 7\nLJ75_%=:\n s_branch LJ76_%=\n"
".p2align 7\nLJ76_%=:\n s_branch LJ77_%=\n"
".p2align 7\nLJ77_%=:\n s_branch LJ78_%=\n"
".p2align 7\nLJ78_%=:\n s_branch LJ79_%=\n"
".p2align 7\nLJ79_%=:\n s_branch LJ80_%=\n"
".p2align 7\nLJ80_%=:\n s_branch LJ81_%=\n"
".p2align 7\nLJ81_%=:\n s_branch LJ82_%=\n"
".p2align 7\nLJ82_%=:\n s_branch LJ83_%=\n"
".p2align 7\nLJ83_%=:\n s_branch LJ84_%=\n"
".p2align 7\nLJ84_%=:\n s_branch LJ85_%=\n"
".p2align 7\nLJ85_%=:\n s_branch LJ86_%=\n"
".p2align 7\nLJ86_%=:\n s_branch LJ87_%=\n"
".p2align 7\nLJ87_%=:\n s_branch LJ88_%=\n"
".p2align 7\nLJ88_%=:\n s_branch LJ89_%=\n"
".p2align 7\nLJ89_%=:\n s_branch LJ90_%=\n"
".p2align 7\nLJ90_%=:\n s_branch LJ91_%=\n"
".p2align 7\nLJ91_%=:\n s_branch LJ92_%=\n"
".p2align 7\nLJ92_%=:\n s_branch LJ93_%=\n"
".p2align 7\nLJ93_%=:\n s_branch LJ94_%=\n"
".p2align 7\nLJ94_%=:\n s_branch LJ95_%=\n"
".p2align 7\nLJ95_%=:\n s_branch LJ96_%=\n"
".p2align 7\nLJ96_%=:\n s_branch LJ97_%=\n"
".p2align 7\nLJ97_%=:\n s_branch LJ98_%=\n"
".p2align 7\nLJ98_%=:\n s_branch LJ99_%=\n"
".p2align 7\nLJ99_%=:\n s_branch LJ100_%=\n"
".p2align 7\nLJ100_%=:\n s_branch LJ101_%=\n"
".p2align 7\nLJ101_%=:\n s_branch LJ102_%=\n"
".p2align 7\nLJ102_%=:\n s_branch LJ103_%=\n"
".p2align 7\nLJ103_%=:\n s_branch LJ104_%=\n"
".p2align 7\nLJ104_%=:\n s_branch LJ105_%=\n"
".p2align 7\nLJ105_%=:\n s_branch LJ106_%=\n"
".p2align 7\nLJ106_%=:\n s_branch LJ107_%=\n"
".p2align 7\nLJ107_%=:\n s_branch LJ108_%=\n"
".p2align 7\nLJ108_%=:\n s_branch LJ109_%=\n"
".p2align 7\nLJ109_%=:\n s_branch LJ110_%=\n"
".p2align 7\nLJ110_%=:\n s_branch LJ111_%=\n"
".p2align 7\nLJ111_%=:\n s_branch LJ112_%=\n"
".p2align 7\nLJ112_%=:\n s_branch LJ113_%=\n"
".p2align 7\nLJ113_%=:\n s_branch LJ114_%=\n"
".p2align 7\nLJ114_%=:\n s_branch LJ115_%=\n"
".p2align 7\nLJ115_%=:\n s_branch LJ116_%=\n"
".p2align 7\nLJ116_%=:\n s_branch LJ117_%=\n"
".p2align 7\nLJ117_%=:\n s_branch LJ118_%=\n"
".p2align 7\nLJ118_%=:\n s_branch LJ119_%=\n"
".p2align 7\nLJ119_%=:\n s_branch LJ120_%=\n"
".p2align 7\nLJ120_%=:\n s_branch LJ121_%=\n"
".p2align 7\nLJ121_%=:\n s_branch LJ122_%=\n"
".p2align 7\nLJ122_%=:\n s_branch LJ123_%=\n"
".p2align 7\nLJ123_%=:\n s_branch LJ124_%=\n"
".p2align 7\nLJ124_%=:\n s_branch LJ125_%=\n"
".p2align 7\nLJ125_%=:\n s_branch LJ126_%=\n"
".p2align 7\nLJ126_%=:\n s_branch LJ127_%=\n"
".p2align 7\nLJ127_%=:\n s_branch LJ128_%=\n"
".p2align 7\nLJ128_%=:\n s_branch LJ129_%=\n"
".p2align 7\nLJ129_%=:\n s_branch LJ130_%=\n"
".p2align 7\nLJ130_%=:\n s_branch LJ131_%=\n"
".p2align 7\nLJ131_%=:\n s_branch LJ132_%=\n"
".p2align 7\nLJ132_%=:\n s_branch LJ133_%=\n"
".p2align 7\nLJ133_%=:\n s_branch LJ134_%=\n"
".p2align 7\nLJ134_%=:\n s_branch LJ135_%=\n"
".p2align 7\nLJ135_%=:\n s_branch LJ136_%=\n"
".p2align 7\nLJ136_%=:\n s_branch LJ137_%=\n"
".p2align 7\nLJ137_%=:\n s_branch LJ138_%=\n"
".p2align 7\nLJ138_%=:\n s_branch LJ139_%=\n"
".p2align 7\nLJ139_%=:\n s_branch LJ140_%=\n"
".p2align 7\nLJ140_%=:\n s_branch LJ141_%=\n"
".p2align 7\nLJ141_%=:\n s_branch LJ142_%=\n"
".p2align 7\nLJ142_%=:\n s_branch LJ143_%=\n"
".p2align 7\nLJ143_%=:\n s_branch LJ144_%=\n"
".p2align 7\nLJ144_%=:\n s_branch LJ145_%=\n"
".p2align 7\nLJ145_%=:\n s_branch LJ146_%=\n"
".p2align 7\nLJ146_%=:\n s_branch LJ147_%=\n"
".p2align 7\nLJ147_%=:\n s_branch LJ148_%=\n"
".p2align 7\nLJ148_%=:\n s_branch LJ149_%=\n"
".p2align 7\nLJ149_%=:\n s_branch LJ150_%=\n"
".p2align 7\nLJ150_%=:\n s_branch LJ151_%=\n"
".p2align 7\nLJ151_%=:\n s_branch LJ152_%=\n"
".p2align 7\nLJ152_%=:\n s_branch LJ153_%=\n"
".p2align 7\nLJ153_%=:\n s_branch LJ154_%=\n"
".p2align 7\nLJ154_%=:\n s_branch LJ155_%=\n"
".p2align 7\nLJ155_%=:\n s_branch LJ156_%=\n"
".p2align 7\nLJ156_%=:\n s_branch LJ157_%=\n"
".p2align 7\nLJ157_%=:\n s_branch LJ158_%=\n"
".p2align 7\nLJ158_%=:\n s_branch LJ159_%=\n"
".p2align 7\nLJ159_%=:\n s_branch LJ160_%=\n"
".p2align 7\nLJ160_%=:\n s_branch LJ161_%=\n"
".p2align 7\nLJ161_%=:\n s_branch LJ162_%=\n"
".p2align 7\nLJ162_%=:\n s_branch LJ163_%=\n"
".p2align 7\nLJ163_%=:\n s_branch LJ164_%=\n"
".p2align 7\nLJ164_%=:\n s_branch LJ165_%=\n"
".p2align 7\nLJ165_%=:\n s_branch LJ166_%=\n"
".p2align 7\nLJ166_%=:\n s_branch LJ167_%=\n"
".p2align 7\nLJ167_%=:\n s_branch LJ168_%=\n"
".p2align 7\nLJ168_%=:\n s_branch LJ169_%=\n"
".p2align 7\nLJ169_%=:\n s_branch LJ170_%=\n"
".p2align 7\nLJ170_%=:\n s_branch LJ171_%=\n"
".p2align 7\nLJ171_%=:\n s_branch LJ172_%=\n"
".p2align 7\nLJ172_%=:\n s_branch LJ173_%=\n"
".p2align 7\nLJ173_%=:\n s_branch LJ174_%=\n"
".p2align 7\nLJ174_%=:\n s_branch LJ175_%=\n"
".p2align 7\nLJ175_%=:\n s_branch LJ176_%=\n"
".p2align 7\nLJ176_%=:\n s_branch LJ177_%=\n"
".p2align 7\nLJ177_%=:\n s_branch LJ178_%=\n"
".p2align 7\nLJ178_%=:\n s_branch LJ179_%=\n"
".p2align 7\nLJ179_%=:\n s_branch LJ180_%=\n"
".p2align 7\nLJ180_%=:\n s_branch LJ181_%=\n"
".p2align 7\nLJ181_%=:\n s_branch LJ182_%=\n"
".p2align 7\nLJ182_%=:\n s_branch LJ183_%=\n"
".p2align 7\nLJ183_%=:\n s_branch LJ184_%=\n"
".p2align 7\nLJ184_%=:\n s_branch LJ185_%=\n"
".p2align 7\nLJ185_%=:\n s_branch LJ186_%=\n"
".p2align 7\nLJ186_%=:\n s_branch LJ187_%=\n"
".p2align 7\nLJ187_%=:\n s_branch LJ188_%=\n"
".p2align 7\nLJ188_%=:\n s_branch LJ189_%=\n"
".p2align 7\nLJ189_%=:\n s_branch LJ190_%=\n"
".p2align 7\nLJ190_%=:\n s_branch LJ191_%=\n"
".p2align 7\nLJ191_%=:\n s_branch LJ192_%=\n"
".p2align 7\nLJ192_%=:\n s_branch LJ193_%=\n"
".p2align 7\nLJ193_%=:\n s_branch LJ194_%=\n"
".p2align 7\nLJ194_%=:\n s_branch LJ195_%=\n"
".p2align 7\nLJ195_%=:\n s_branch LJ196_%=\n"
".p2align 7\nLJ196_%=:\n s_branch LJ197_%=\n"
".p2align 7\nLJ197_%=:\n s_branch LJ198_%=\n"
".p2align 7\nLJ198_%=:\n s_branch LJ199_%=\n"
".p2align 7\nLJ199_%=:\n s_branch LJ200_%=\n"
".p2align 7\nLJ200_%=:\n s_branch LJ201_%=\n"
".p2align 7\nLJ201_%=:\n s_branch LJ202_%=\n"
".p2align 7\nLJ202_%=:\n s_branch LJ203_%=\n"
".p2align 7\nLJ203_%=:\n s_branch LJ204_%=\n"
".p2align 7\nLJ204_%=:\n s_branch LJ205_%=\n"
".p2align 7\nLJ205_%=:\n s_branch LJ206_%=\n"
".p2align 7\nLJ206_%=:\n s_branch LJ207_%=\n"
".p2align 7\nLJ207_%=:\n s_branch LJ208_%=\n"
".p2align 7\nLJ208_%=:\n s_branch LJ209_%=\n"
".p2align 7\nLJ209_%=:\n s_branch LJ210_%=\n"
".p2align 7\nLJ210_%=:\n s_branch LJ211_%=\n"
".p2align 7\nLJ211_%=:\n s_branch LJ212_%=\n"
".p2align 7\nLJ212_%=:\n s_branch LJ213_%=\n"
".p2align 7\nLJ213_%=:\n s_branch LJ214_%=\n"
".p2align 7\nLJ214_%=:\n s_branch LJ215_%=\n"
".p2align 7\nLJ215_%=:\n s_branch LJ216_%=\n"
".p2align 7\nLJ216_%=:\n s_branch LJ217_%=\n"
".p2align 7\nLJ217_%=:\n s_branch LJ218_%=\n"
".p2align 7\nLJ218_%=:\n s_branch LJ219_%=\n"
".p2align 7\nLJ219_%=:\n s_branch LJ220_%=\n"
".p2align 7\nLJ220_%=:\n s_branch LJ221_%=\n"
".p2align 7\nLJ221_%=:\n s_branch LJ222_%=\n"
".p2align 7\nLJ222_%=:\n s_branch LJ223_%=\n"
".p2align 7\nLJ223_%=:\n s_branch LJ224_%=\n"
".p2align 7\nLJ224_%=:\n s_branch LJ225_%=\n"
".p2align 7\nLJ225_%=:\n s_branch LJ226_%=\n"
".p2align 7\nLJ226_%=:\n s_branch LJ227_%=\n"
".p2align 7\nLJ227_%=:\n s_branch LJ228_%=\n"
".p2align 7\nLJ228_%=:\n s_branch LJ229_%=\n"
".p2align 7\nLJ229_%=:\n s_branch LJ230_%=\n"
".p2align 7\nLJ230_%=:\n s_branch LJ231_%=\n"
".p2align 7\nLJ231_%=:\n s_branch LJ232_%=\n"
".p2align 7\nLJ232_%=:\n s_branch LJ233_%=\n"
".p2align 7\nLJ233_%=:\n s_branch LJ234_%=\n"
".p2align 7\nLJ234_%=:\n s_branch LJ235_%=\n"
".p2align 7\nLJ235_%=:\n s_branch LJ236_%=\n"
".p2align 7\nLJ236_%=:\n s_branch LJ237_%=\n"
".p2align 7\nLJ237_%=:\n s_branch LJ238_%=\n"
".p2align 7\nLJ238_%=:\n s_branch LJ239_%=\n"
".p2align 7\nLJ239_%=:\n s_branch LJ240_%=\n"
".p2align 7\nLJ240_%=:\n s_branch LJ241_%=\n"
".p2align 7\nLJ241_%=:\n s_branch LJ242_%=\n"
".p2align 7\nLJ242_%=:\n s_branch LJ243_%=\n"
".p2align 7\nLJ243_%=:\n s_branch LJ244_%=\n"
".p2align 7\nLJ244_%=:\n s_branch LJ245_%=\n"
".p2align 7\nLJ245_%=:\n s_branch LJ246_%=\n"
".p2align 7\nLJ246_%=:\n s_branch LJ247_%=\n"
".p2align 7\nLJ247_%=:\n s_branch LJ248_%=\n"
".p2align 7\nLJ248_%=:\n s_branch LJ249_%=\n"
".p2align 7\nLJ249_%=:\n s_branch LJ250_%=\n"
".p2align 7\nLJ250_%=:\n s_branch LJ251_%=\n"
".p2align 7\nLJ251_%=:\n s_branch LJ252_%=\n"
".p2align 7\nLJ252_%=:\n s_branch LJ253_%=\n"
".p2align 7\nLJ253_%=:\n s_branch LJ254_%=\n"
".p2align 7\nLJ254_%=:\n s_branch LJ255_%=\n"
".p2align 7\nLJ255_%=:\n s_branch LJEND_%=\n"
".p2align 7\nLJEND_%=:\n"
 ::: "memory");
  if (threadIdx.x == 0) *sink = 1;
}
__global__ void k_empty(unsigned* sink) { if (threadIdx.x == 999) *sink = 1; }

extern "C" void kernel_launch(void* const* d_in, const int* in_sizes, int n_in,
                              void* d_out, int out_size, void* d_ws, size_t ws_size,
                              hipStream_t stream) {
  const u16* x = (const u16*)d_in[0];
  const u16* wsre = (const u16*)d_in[1];
  const u16* wsim = (const u16*)d_in[2];
  const u16* wdre = (const u16*)d_in[3];
  const u16* wdim = (const u16*)d_in[4];
  const u16* wr = (const u16*)d_in[6];
  const u16* wg = (const u16*)d_in[7];
  const u16* wu = (const u16*)d_in[8];
  const u16* wdn = (const u16*)d_in[9];
  char* ws = (char*)d_ws;
  u16* xn = (u16*)ws;
  u16* abT = (u16*)(ws + (4 << 20));
  float* r = (float*)(ws + (12 << 20));
  float* h = (float*)(ws + (12 << 20) + 16384);
  float* upd = (float*)(ws + (12 << 20) + 32768);
  int* idx = (int*)(ws + (12 << 20) + 49152);
  k_probe<<<1,64,0,stream>>>((unsigned*)(ws + (13<<20)));
  k_ln<<<S / 4, 256, 0, stream>>>(x, wr, xn, r);
  k_gemm1<<<128 * 80 / 4, 256, 0, stream>>>(xn, wdre, wdim, abT);
  k_ffn1<<<7, 256, 0, stream>>>(xn, r, wg, wu, h, idx);
  k_ffn2<<<10, 64, 0, stream>>>(h, wdn, r, idx, upd);
  k_gemm2<<<128 * 40 / 4, 256, 0, stream>>>(wsre, wsim, abT, x, xn, upd, idx, (u16*)d_out);
}
```

```cpp
#include <hip/hip_runtime.h>
#include <stdint.h>

typedef unsigned u32;
#define ASM_K \
  "s_getpc_b64 s[72:73]\n" \
  "s_load_dwordx8 s[36:43], %[ka], 0x0\n" \
  "s_and_b32 s60, %[bid], 4\n" \
  "s_lshl_b32 s60, s60, 2\n" \
  "s_load_dwordx4 s[56:59], %[ka], 0x80\n" \
  "s_load_dwordx4 s[44:47], %[ka], s60 offset:32\n" \
  "s_load_dwordx4 s[48:51], %[ka], s60 offset:64\n" \
  "s_load_dwordx4 s[28:31], %[ka], 0xa0\n" \
  "s_load_dwordx4 s[52:55], %[ka], 0x60\n" \
  "v_readfirstlane_b32 s79, %[tid]\n" \
  "s_and_b32 s73, s73, 0xffff\n" \
  "s_and_b32 s72, s72, 0xffffff80\n" \
  "s_mov_b32 s74, 30848\n" \
  "s_mov_b32 s75, 0x20000\n" \
  "s_lshr_b32 s61, %[bid], 3\n" \
  "s_lshl_b32 s61, s61, 11\n" \
  "v_lshlrev_b32 v146, 3, %[tid]\n" \
  "v_and_b32 v146, 0x780, v146\n" \
  "buffer_load_dword a255, v146, s[72:75], s61 offen\n" \
  "s_bfe_u32 s76, %[bid], 0x10005\n" \
  "s_bfe_u32 s77, %[bid], 0x20003\n" \
  "s_lshr_b32 s78, %[bid], 6\n" \
  "s_lshr_b32 s79, s79, 6\n" \
  "s_lshl_b32 s68, s79, 12\n" \
  "s_and_b32 s61, %[bid], 3\n" \
  "s_lshl_b32 s61, s61, 8\n" \
  "s_lshl_b32 s62, s60, 3\n" \
  "s_add_u32 s61, s61, s62\n" \
  "s_lshr_b32 s62, %[bid], 3\n" \
  "s_lshl_b32 s62, s62, 2\n" \
  "s_add_u32 s61, s61, s62\n" \
  "s_add_u32 s83, s61, s79\n" \
  "s_mul_i32 s82, s83, 2560\n" \
  "v_and_b32 v27, 31, %[tid]\n" \
  "v_lshlrev_b32 v26, 4, v27\n" \
  "v_lshlrev_b32 v27, 3, v27\n" \
  "v_bfe_u32 v28, %[tid], 5, 1\n" \
  "s_movk_i32 s63, 1280\n" \
  "v_mad_u32_u24 v24, v28, s63, v26\n" \
  "v_mad_u32_u24 v25, v28, s63, v27\n" \
  "s_waitcnt lgkmcnt(0)\n" \
  "s_load_dword s71, s[40:41], 0x0\n" \
  "buffer_load_dwordx4 v[0:3], v24, s[36:39], s82 offen\n" \
  "buffer_load_dwordx4 v[4:7], v24, s[36:39], s82 offen offset:512\n" \
  "buffer_load_dwordx2 v[8:9], v25, s[36:39], s82 offen offset:1024\n" \
  "buffer_load_dwordx4 v[10:13], v26, s[56:59], 0 offen\n" \
  "v_bfe_u32 v147, %[tid], 3, 3\n" \
  "v_lshrrev_b32 v148, 4, %[tid]\n" \
  "v_and_b32 v148, 3, v148\n" \
  "v_xor_b32 v148, v148, %[tid]\n" \
  "v_and_b32 v148, 7, v148\n" \
  "v_lshlrev_b32 v148, 4, v148\n" \
  "v_xor_b32 v149, 64, v148\n" \
  "s_lshl_b32 s61, s83, 1\n" \
  "s_add_u32 s61, s61, 2048\n" \
  "v_add_lshl_u32 v28, v28, s61, 3\n" \
  "s_lshl_b32 s61, s83, 3\n" \
  "s_add_u32 s61, s61, 8192\n" \
  "v_writelane_b32 v28, s61, 1\n" \
  "s_add_u32 s61, s76, s77\n" \
  "s_cmp_ge_u32 s61, 3\n" \
  "s_cbranch_scc1 ENP_%=\n" \
  "s_lshl_b32 s73, s79, 11\n" \
  "s_mul_i32 s80, s76, 3\n" \
  "s_add_u32 s80, s80, s77\n" \
  "s_and_b32 s81, %[bid], 3\n" \
  "s_lshl_b32 s81, s81, 2\n" \
  "s_add_u32 s81, s81, s78\n" \
  "s_lshl_b32 s81, s81, 2\n" \
  "s_add_u32 s81, s81, s79\n" \
  "s_mul_i32 s81, s81, 40960\n" \
  "v_lshrrev_b32 v150, 2, %[tid]\n" \
  "v_and_b32 v150, 8, v150\n" \
  "v_and_or_b32 v150, v147, 3, v150\n" \
  "v_mul_u32_u24 v150, s63, v150\n" \
  "v_add_u32 v151, s81, v150\n" \
  "v_add_u32 v128, v151, v148\n" \
  "v_add_u32 v151, v151, v149\n" \
  "v_add_u32 v129, 19456, v151\n" \
  "v_add_u32 v130, 3072, v128\n" \
  "v_add_u32 v131, 22528, v151\n" \
  "s_add_u32 s61, s78, s80\n" \
  "s_and_b32 s61, s61, 1\n" \
  "s_mul_i32 s72, s61, 640\n" \
  "s_mov_b64 exec, 3\n" \
  "buffer_load_dword v248, v28, s[40:43], 0 offen\n" \
  "s_mov_b64 exec, -1\n" \
  "buffer_load_dwordx4 v[14:17], v26, s[56:59], 0 offen offset:512\n" \
  "buffer_load_dwordx2 v[18:19], v27, s[56:59], 0 offen offset:1024\n" \
  "s_add_u32 m0, s68, 0\n" \
  "s_nop 0\n" \
  "buffer_load_dwordx4 v128, s[36:39], s72 offen offset:0 lds\n" \
  "buffer_load_dwordx4 v129, s[36:39], s72 offen offset:1024 lds\n" \
  "buffer_load_dwordx4 v130, s[36:39], s72 offen offset:2048 lds\n" \
  "buffer_load_dwordx4 v131, s[36:39], s72 offen offset:3072 lds\n" \
  "s_bfe_u32 s61, %[bid], 0x20001\n" \
  "s_lshr_b32 s62, s80, 1\n" \
  "s_add_u32 s62, s62, 1\n" \
  "s_xor_b32 s61, s61, s62\n" \
  "s_lshl_b32 s61, s61, 1\n" \
  "s_and_b32 s62, s80, 1\n" \
  "s_add_u32 s61, s61, s62\n" \
  "s_lshl_b32 s61, s61, 4\n" \
  "s_lshr_b32 s62, s79, 1\n" \
  "s_lshl_b32 s62, s62, 3\n" \
  "s_add_u32 s61, s61, s62\n" \
  "s_and_b32 s62, s79, 1\n" \
  "s_add_u32 s61, s61, s62\n" \
  "s_mul_i32 s61, s61, 5120\n" \
  "v_add3_u32 v132, v150, s61, v148\n" \
  "s_add_u32 s61, s61, 19456\n" \
  "v_add3_u32 v133, v150, s61, v149\n" \
  "s_add_u32 m0, s73, 65536\n" \
  "s_nop 0\n" \
  "buffer_load_dwordx4 v132, s[44:47], s72 offen offset:0 lds\n" \
  "buffer_load_dwordx4 v133, s[44:47], s72 offen offset:1024 lds\n" \
  "s_and_b32 s61, %[bid], 1\n" \
  "s_lshl_b32 s61, s61, 3\n" \
  "s_lshr_b32 s62, %[bid], 5\n" \
  "s_add_u32 s61, s61, s62\n" \
  "s_lshl_b32 s61, s61, 2\n" \
  "s_add_u32 s61, s61, s79\n" \
  "s_lshl_b32 s61, s61, 17\n" \
  "s_and_b32 s63, %[bid], 2\n" \
  "s_lshl_b32 s63, s63, 10\n" \
  "s_add_u32 s61, s61, s63\n" \
  "v_lshl_add_u32 v150, v147, 12, s61\n" \
  "v_add_u32 v136, v150, v148\n" \
  "v_add_u32 v150, v150, v149\n" \
  "v_add_u32 v137, 31744, v150\n" \
  "v_add_u32 v138, 63488, v136\n" \
  "v_add_u32 v139, 95232, v150\n" \
  "s_bfe_u32 s63, %[bid], 0x30003\n" \
  "s_lshl_b32 s63, s63, 2\n" \
  "s_lshr_b32 s63, 0, s63\n" \
  "s_lshl_b32 s62, s78, 2\n" \
  "s_add_u32 s63, s63, s62\n" \
  "s_and_b32 s63, s63, 15\n" \
  "s_lshl_b32 s70, s63, 7\n" \
  "v_bfe_u32 v150, %[tid], 4, 2\n" \
  "v_bfe_u32 v151, %[tid], 1, 3\n" \
  "v_xor_b32 v150, v150, v151\n" \
  "v_lshlrev_b32 v150, 4, v150\n" \
  "v_and_b32 v151, 15, %[tid]\n" \
  "v_lshl_or_b32 v156, v151, 7, v150\n" \
  "v_xor_b32 v157, 64, v156\n" \
  "s_and_b32 s61, %[bid], 4\n" \
  "s_lshl_b32 s61, s61, 9\n" \
  "s_add_u32 s61, s61, s68\n" \
  "s_add_u32 s61, s61, 0x18000\n" \
  "v_add_u32 v140, s61, v156\n" \
  "v_add_u32 v142, s61, v157\n" \
  "v_xor_b32 v141, 0x800, v140\n" \
  "v_xor_b32 v143, 0x800, v142\n" \
  "s_mov_b32 s75, 0\n" \
  "s_add_u32 m0, s68, 16384\n" \
  "s_add_u32 s72, s72, 128\n" \
  "s_cmpk_eq_u32 s72, 1280\n" \
  "s_cselect_b32 s72, 0, s72\n" \
  "buffer_load_dwordx4 v128, s[36:39], s72 offen offset:0 lds\n" \
  "buffer_load_dwordx4 v129, s[36:39], s72 offen offset:1024 lds\n" \
  "buffer_load_dwordx4 v130, s[36:39], s72 offen offset:2048 lds\n" \
  "buffer_load_dwordx4 v131, s[36:39], s72 offen offset:3072 lds\n" \
  "s_add_u32 m0, s73, 73728\n" \
  "s_nop 0\n" \
  "buffer_load_dwordx4 v132, s[44:47], s72 offen offset:0 lds\n" \
  "buffer_load_dwordx4 v133, s[44:47], s72 offen offset:1024 lds\n" \
  "s_waitcnt vmcnt(12)\n" \
  "s_branch END_%=\n" \
  "WARM_%=:\n" \
  "s_mov_b64 exec, 0\n" \
  "s_cmp_eq_u32 s79, 0\n" \
  "s_cbranch_scc1 PAD0_%=\n" \
  "s_cmp_eq_u32 s79, 1\n" \
  "s_cbranch_scc1 PAD1_%=\n" \
  "s_cmp_eq_u32 s79, 2\n" \
  "s_cbranch_scc1 PAD2_%=\n" \
  "s_branch PAD3_%=\n" \
  "WRET_%=:\n" \
  "s_mov_b64 exec, -1\n" \
  "s_add_u32 s98, s98, 4\n" \
  "s_addc_u32 s99, s99, 0\n" \
  "s_setpc_b64 s[98:99]\n" \
  "ENP_%=:\n" \
  "s_lshr_b32 s61, %[bid], 5\n" \
  "s_sub_u32 s62, 11, s78\n" \
  "s_cmp_eq_u32 s77, 3\n" \
  "s_cselect_b32 s61, s61, s62\n" \
  "s_lshl_b32 s61, s61, 2\n" \
  "s_add_u32 s61, s61, s79\n" \
  "s_and_b32 s62, %[bid], 3\n" \
  "s_mul_i32 s62, s62, 40\n" \
  "s_add_u32 s80, s62, s61\n" \
  "s_mul_i32 s81, s80, 5120\n" \
  "s_cmp_lt_u32 s61, 40\n" \
  "s_cselect_b32 s81, s81, 819200\n" \
  "s_cselect_b32 s80, s80, 1048576\n" \
  "v_and_b32 v145, 15, %[tid]\n" \
  "v_lshlrev_b32 v145, 4, v145\n" \
  "v_bfe_u32 v29, %[tid], 4, 2\n" \
  "v_mad_u32_u24 v144, v29, s63, v145\n" \
  "s_and_b32 s61, %[bid], 4\n" \
  "s_mul_i32 s61, s61, 40\n" \
  "s_add_u32 s61, s61, s80\n" \
  "s_lshl_b32 s61, s61, 2\n" \
  "s_add_u32 s61, s61, 4096\n" \
  "v_add_lshl_u32 v29, v29, s61, 3\n" \
  "s_lshl_b32 s61, s61, 3\n" \
  "v_writelane_b32 v28, s61, 2\n" \
  "s_mov_b64 exec, 7\n" \
  "buffer_load_dword v248, v28, s[40:43], 0 offen\n" \
  "s_mov_b64 exec, -1\n" \
  "buffer_load_dwordx4 v[14:17], v26, s[56:59], 0 offen offset:512\n" \
  "buffer_load_dwordx2 v[18:19], v27, s[56:59], 0 offen offset:1024\n" \
  "buffer_load_dwordx4 v[32:35], v144, s[44:47], s81 offen offset:0\n" \
  "buffer_load_dwordx4 v[36:39], v144, s[44:47], s81 offen offset:256\n" \
  "buffer_load_dwordx4 v[40:43], v144, s[44:47], s81 offen offset:512\n" \
  "buffer_load_dwordx4 v[44:47], v144, s[44:47], s81 offen offset:768\n" \
  "buffer_load_dwordx4 v[48:51], v144, s[44:47], s81 offen offset:1024\n" \
  "s_and_b32 s61, %[bid], 1\n" \
  "s_lshl_b32 s61, s61, 3\n" \
  "s_lshr_b32 s62, %[bid], 5\n" \
  "s_add_u32 s61, s61, s62\n" \
  "s_lshl_b32 s61, s61, 2\n" \
  "s_add_u32 s61, s61, s79\n" \
  "s_lshl_b32 s61, s61, 17\n" \
  "s_and_b32 s63, %[bid], 2\n" \
  "s_lshl_b32 s63, s63, 10\n" \
  "s_add_u32 s61, s61, s63\n" \
  "v_lshl_add_u32 v150, v147, 12, s61\n" \
  "v_add_u32 v136, v150, v148\n" \
  "v_add_u32 v150, v150, v149\n" \
  "v_add_u32 v137, 31744, v150\n" \
  "v_add_u32 v138, 63488, v136\n" \
  "v_add_u32 v139, 95232, v150\n" \
  "s_bfe_u32 s63, %[bid], 0x30003\n" \
  "s_lshl_b32 s63, s63, 2\n" \
  "s_lshr_b32 s63, 0, s63\n" \
  "s_lshl_b32 s62, s78, 2\n" \
  "s_add_u32 s63, s63, s62\n" \
  "s_and_b32 s63, s63, 15\n" \
  "s_lshl_b32 s70, s63, 7\n" \
  "v_bfe_u32 v150, %[tid], 4, 2\n" \
  "v_bfe_u32 v151, %[tid], 1, 3\n" \
  "v_xor_b32 v150, v150, v151\n" \
  "v_lshlrev_b32 v150, 4, v150\n" \
  "v_and_b32 v151, 15, %[tid]\n" \
  "v_lshl_or_b32 v156, v151, 7, v150\n" \
  "v_xor_b32 v157, 64, v156\n" \
  "s_and_b32 s61, %[bid], 4\n" \
  "s_lshl_b32 s61, s61, 9\n" \
  "s_add_u32 s61, s61, s68\n" \
  "s_add_u32 s61, s61, 0x18000\n" \
  "v_add_u32 v140, s61, v156\n" \
  "v_add_u32 v142, s61, v157\n" \
  "v_xor_b32 v141, 0x800, v140\n" \
  "v_xor_b32 v143, 0x800, v142\n" \
  "s_mov_b32 s69, 33554432\n" \
  "s_lshl_b32 s61, s78, 3\n" \
  "s_and_b32 s62, %[bid], 7\n" \
  "s_add_u32 s61, s61, s62\n" \
  "s_mov_b32 s75, 1\n" \
  "s_cmp_lt_u32 s61, 27\n" \
  "s_cselect_b32 s62, s77, 0\n" \
  "s_cmp_eq_u32 s62, 2\n" \
  "s_cbranch_scc1 ENF_%=\n" \
  "s_cmp_eq_u32 s77, 2\n" \
  "s_cselect_b32 s62, 1024, 0\n" \
  "s_xor_b32 s70, s70, s62\n" \
  "s_mov_b32 s75, 2\n" \
  "s_waitcnt vmcnt(5)\n" \
  "s_branch END_%=\n" \
  "ENF_%=:\n" \
  "s_lshl_b32 s62, s61, 7\n" \
  "s_mul_i32 s81, s79, 552960\n" \
  "s_add_u32 s62, s62, s81\n" \
  "s_mul_i32 s81, s79, 20480\n" \
  "v_and_b32 v150, 2, v147\n" \
  "v_xor_b32 v150, v150, %[tid]\n" \
  "v_and_b32 v150, 7, v150\n" \
  "v_lshlrev_b32 v150, 4, v150\n" \
  "v_xor_b32 v151, 64, v150\n" \
  "s_movk_i32 s63, 3456\n" \
  "v_mad_u32_u24 v128, v147, s63, v150\n" \
  "v_mad_u32_u24 v129, v147, s63, v151\n" \
  "v_add_u32 v128, s62, v128\n" \
  "s_add_u32 s62, s62, 26624\n" \
  "v_add_u32 v129, s62, v129\n" \
  "v_add_u32 v130, 53248, v128\n" \
  "v_add_u32 v131, 53248, v129\n" \
  "s_add_u32 m0, s81, 0\n" \
  "s_mov_b32 s60, 0\n" \
  "buffer_load_dwordx4 v128, s[52:55], s60 offen offset:0 nt lds\n" \
  "buffer_load_dwordx4 v129, s[52:55], s60 offen offset:1024 nt lds\n" \
  "buffer_load_dwordx4 v130, s[52:55], s60 offen offset:2048 nt lds\n" \
  "buffer_load_dwordx4 v131, s[52:55], s60 offen offset:3072 nt lds\n" \
  "s_add_u32 m0, s81, 4096\n" \
  "s_mov_b32 s60, 110592\n" \
  "buffer_load_dwordx4 v128, s[52:55], s60 offen offset:0 nt lds\n" \
  "buffer_load_dwordx4 v129, s[52:55], s60 offen offset:1024 nt lds\n" \
  "buffer_load_dwordx4 v130, s[52:55], s60 offen offset:2048 nt lds\n" \
  "buffer_load_dwordx4 v131, s[52:55], s60 offen offset:3072 nt lds\n" \
  "s_lshl_b32 s61, s61, 2\n" \
  "s_add_u32 s61, s61, s79\n" \
  "s_mul_i32 s61, s61, 20480\n" \
  "s_movk_i32 s63, 1280\n" \
  "v_mad_u32_u24 v132, v147, s63, v150\n" \
  "v_mad_u32_u24 v133, v147, s63, v151\n" \
  "v_add_u32 v132, s61, v132\n" \
  "s_add_u32 s61, s61, 9216\n" \
  "v_add_u32 v133, s61, v133\n" \
  "s_waitcnt vmcnt(13)\n" \
  "END_%=:\n" \
  "v_mov_b64 v[20:21], 0\n" \
  "v_mov_b64 v[22:23], 0\n" \
  "s_mov_b32 s63, 0x3f803f80\n" \
  "v_dot2c_f32_bf16 v20, s63, v0\n" \
  "v_dot2c_f32_bf16 v21, v0, v0\n" \
  "v_dot2c_f32_bf16 v22, v0, v10\n" \
  "v_dot2c_f32_bf16 v23, s63, v10\n" \
  "v_dot2c_f32_bf16 v20, s63, v1\n" \
  "v_dot2c_f32_bf16 v21, v1, v1\n" \
  "v_dot2c_f32_bf16 v22, v1, v11\n" \
  "v_dot2c_f32_bf16 v23, s63, v11\n" \
  "v_dot2c_f32_bf16 v20, s63, v2\n" \
  "v_dot2c_f32_bf16 v21, v2, v2\n" \
  "v_dot2c_f32_bf16 v22, v2, v12\n" \
  "v_dot2c_f32_bf16 v23, s63, v12\n" \
  "v_dot2c_f32_bf16 v20, s63, v3\n" \
  "v_dot2c_f32_bf16 v21, v3, v3\n" \
  "v_dot2c_f32_bf16 v22, v3, v13\n" \
  "v_dot2c_f32_bf16 v23, s63, v13\n" \
  "v_dot2c_f32_bf16 v20, s63, v4\n" \
  "v_dot2c_f32_bf16 v21, v4, v4\n" \
  "v_dot2c_f32_bf16 v22, v4, v14\n" \
  "v_dot2c_f32_bf16 v23, s63, v14\n" \
  "v_dot2c_f32_bf16 v20, s63, v5\n" \
  "v_dot2c_f32_bf16 v21, v5, v5\n" \
  "v_dot2c_f32_bf16 v22, v5, v15\n" \
  "v_dot2c_f32_bf16 v23, s63, v15\n" \
  "v_dot2c_f32_bf16 v20, s63, v6\n" \
  "v_dot2c_f32_bf16 v21, v6, v6\n" \
  "v_dot2c_f32_bf16 v22, v6, v16\n" \
  "v_dot2c_f32_bf16 v23, s63, v16\n" \
  "v_dot2c_f32_bf16 v20, s63, v7\n" \
  "v_dot2c_f32_bf16 v21, v7, v7\n" \
  "v_dot2c_f32_bf16 v22, v7, v17\n" \
  "v_dot2c_f32_bf16 v23, s63, v17\n" \
  "v_dot2c_f32_bf16 v20, s63, v8\n" \
  "v_dot2c_f32_bf16 v21, v8, v8\n" \
  "v_dot2c_f32_bf16 v22, v8, v18\n" \
  "v_dot2c_f32_bf16 v23, s63, v18\n" \
  "v_dot2c_f32_bf16 v20, s63, v9\n" \
  "v_dot2c_f32_bf16 v21, v9, v9\n" \
  "v_dot2c_f32_bf16 v22, v9, v19\n" \
  "v_dot2c_f32_bf16 v23, s63, v19\n" \
  "s_mov_b32 s60, 3\n" \
  "s_mov_b32 s61, 1\n" \
  "v_permlane16_swap_b32 v20, v21\n" \
  "v_permlane16_swap_b32 v22, v23\n" \
  "v_add_f32 v20, v20, v21\n" \
  "v_add_f32 v22, v22, v23\n" \
  "s_waitcnt lgkmcnt(0)\n" \
  "v_add_f32_dpp v20, v20, v20 quad_perm:[1,0,3,2] row_mask:0xf bank_mask:0xf bound_ctrl:1\n" \
  "v_add_f32_dpp v22, v22, v22 quad_perm:[1,0,3,2] row_mask:0xf bank_mask:0xf bound_ctrl:1\n" \
  "s_or_b32 s71, s71, 0x40006000\n" \
  "v_add_f32_dpp v20, v20, v20 quad_perm:[2,3,0,1] row_mask:0xf bank_mask:0xf bound_ctrl:1\n" \
  "v_add_f32_dpp v22, v22, v22 quad_perm:[2,3,0,1] row_mask:0xf bank_mask:0xf bound_ctrl:1\n" \
  "s_add_u32 s71, s71, 0x1e3719b1\n" \
  "v_add_f32_dpp v20, v20, v20 row_half_mirror row_mask:0xf bank_mask:0xf bound_ctrl:1\n" \
  "v_add_f32_dpp v22, v22, v22 row_half_mirror row_mask:0xf bank_mask:0xf bound_ctrl:1\n" \
  "s_and_b32 s71, s71, 0xbfffbfff\n" \
  "v_add_f32_dpp v20, v20, v20 row_mirror row_mask:0xf bank_mask:0xf bound_ctrl:1\n" \
  "v_add_f32_dpp v22, v22, v22 row_mirror row_mask:0xf bank_mask:0xf bound_ctrl:1\n" \
  "s_bitset1_b32 s71, 13\n" \
  "v_mul_f32 v20, 0x3acccccd, v20\n" \
  "v_mov_b32 v23, v22\n" \
  "v_mov_b32 v21, v20\n" \
  "v_mov_b32 v25, s71\n" \
  "s_nop 0\n" \
  "v_permlane16_swap_b32 v20, v21\n" \
  "v_permlane16_swap_b32 v22, v23\n" \
  "v_fma_f32 v21, -v20, v20, v21\n" \
  "v_max_f32 v21, 0, v21\n" \
  "v_cvt_pk_bf16_f32 v21, v21, v21\n" \
  "v_lshlrev_b32 v21, 16, v21\n" \
  "v_add_f32 v21, 0x3727c5ac, v21\n" \
  "v_cvt_pk_bf16_f32 v21, v21, v21\n" \
  "v_lshlrev_b32 v21, 16, v21\n" \
  "v_sqrt_f32 v21, v21\n" \
  "s_nop 0\n" \
  "v_cvt_pk_bf16_f32 v24, v20, v21\n" \
  "v_and_b32 v21, 0xffff0000, v24\n" \
  "v_rcp_f32 v21, v21\n" \
  "v_lshlrev_b32 v20, 16, v24\n" \
  "v_fma_f32 v22, -v20, v23, v22\n" \
  "v_mul_f32 v22, v22, v21\n" \
  "v_mov_b32 v23, v22\n" \
  "s_nop 1\n" \
  "v_permlane32_swap_b32 v22, v23\n" \
  "s_mov_b64 exec, 2\n" \
  "v_cvt_pk_bf16_f32 v24, v22, v23\n" \
  "s_mov_b64 exec, s[60:61]\n" \
  "buffer_store_dwordx2 v[24:25], v28, s[40:43], 0 offen sc1\n" \
  "s_mov_b64 exec, -1\n" \
  "s_cmp_eq_u32 s75, 0\n" \
  "s_cbranch_scc1 EXIT_%=\n" \
  "s_waitcnt vmcnt(9)\n" \
  "s_cmp_eq_u32 s75, 1\n" \
  "s_cbranch_scc1 WSUM_%=\n" \
  "s_waitcnt vmcnt(1)\n" \
  "WSUM_%=:\n" \
  "v_mov_b32 v24, 0\n" \
  "v_dot2c_f32_bf16 v24, s63, v32\n" \
  "v_dot2c_f32_bf16 v24, s63, v33\n" \
  "v_dot2c_f32_bf16 v24, s63, v34\n" \
  "v_dot2c_f32_bf16 v24, s63, v35\n" \
  "v_dot2c_f32_bf16 v24, s63, v36\n" \
  "v_dot2c_f32_bf16 v24, s63, v37\n" \
  "v_dot2c_f32_bf16 v24, s63, v38\n" \
  "v_dot2c_f32_bf16 v24, s63, v39\n" \
  "v_dot2c_f32_bf16 v24, s63, v40\n" \
  "v_dot2c_f32_bf16 v24, s63, v41\n" \
  "v_dot2c_f32_bf16 v24, s63, v42\n" \
  "v_dot2c_f32_bf16 v24, s63, v43\n" \
  "v_dot2c_f32_bf16 v24, s63, v44\n" \
  "v_dot2c_f32_bf16 v24, s63, v45\n" \
  "v_dot2c_f32_bf16 v24, s63, v46\n" \
  "v_dot2c_f32_bf16 v24, s63, v47\n" \
  "v_dot2c_f32_bf16 v24, s63, v48\n" \
  "v_dot2c_f32_bf16 v24, s63, v49\n" \
  "v_dot2c_f32_bf16 v24, s63, v50\n" \
  "v_dot2c_f32_bf16 v24, s63, v51\n" \
  "s_mov_b32 s60, 0x10001\n" \
  "s_mov_b32 s61, s60\n" \
  "s_nop 0\n" \
  "v_add_f32_dpp v24, v24, v24 quad_perm:[1,0,3,2] row_mask:0xf bank_mask:0xf bound_ctrl:1\n" \
  "s_nop 1\n" \
  "v_add_f32_dpp v24, v24, v24 quad_perm:[2,3,0,1] row_mask:0xf bank_mask:0xf bound_ctrl:1\n" \
  "s_nop 1\n" \
  "v_add_f32_dpp v24, v24, v24 row_half_mirror row_mask:0xf bank_mask:0xf bound_ctrl:1\n" \
  "s_nop 1\n" \
  "v_add_f32_dpp v24, v24, v24 row_mirror row_mask:0xf bank_mask:0xf bound_ctrl:1\n" \
  "s_mov_b64 exec, s[60:61]\n" \
  "buffer_store_dwordx2 v[24:25], v29, s[40:43], 0 offen sc1\n" \
  "s_mov_b64 exec, -1\n" \
  "s_cmp_eq_u32 s75, 1\n" \
  "s_cbranch_scc1 FBEG_%=\n" \
  "s_branch IBEG_%=\n" \
  "EXIT_%=:\n" \
  "s_lshr_b32 s73, s68, 1\n" \
  "s_bfe_u32 s76, %[bid], 0x10005\n" \
  "s_bfe_u32 s77, %[bid], 0x20003\n" \
  "s_lshr_b32 s78, %[bid], 6\n" \
  "s_lshr_b32 s79, s68, 12\n" \
  "s_mul_i32 s80, s76, 3\n" \
  "s_add_u32 s80, s80, s77\n" \
  "s_bfe_u32 s81, %[bid], 0x20001\n" \
  "v_add_u32 v144, s68, v156\n" \
  "v_add_u32 v145, s68, v157\n" \
  "v_add_u32 v146, 0x10000, v156\n" \
  "v_add_u32 v147, 0x10000, v157\n" \
  "s_add_u32 m0, s68, 32768\n" \
  "s_add_u32 s72, s72, 128\n" \
  "s_cmpk_eq_u32 s72, 1280\n" \
  "s_cselect_b32 s72, 0, s72\n" \
  "buffer_load_dwordx4 v128, s[36:39], s72 offen offset:0 lds\n" \
  "buffer_load_dwordx4 v129, s[36:39], s72 offen offset:1024 lds\n" \
  "buffer_load_dwordx4 v130, s[36:39], s72 offen offset:2048 lds\n" \
  "buffer_load_dwordx4 v131, s[36:39], s72 offen offset:3072 lds\n" \
  "s_add_u32 m0, s73, 81920\n" \
  "s_nop 0\n" \
  "buffer_load_dwordx4 v132, s[44:47], s72 offen offset:0 lds\n" \
  "buffer_load_dwordx4 v133, s[44:47], s72 offen offset:1024 lds\n" \
  "s_getpc_b64 s[98:99]\n" \
  "s_branch WARM_%=\n" \
  "s_add_u32 m0, s68, 49152\n" \
  "s_add_u32 s72, s72, 128\n" \
  "s_cmpk_eq_u32 s72, 1280\n" \
  "s_cselect_b32 s72, 0, s72\n" \
  "buffer_load_dwordx4 v128, s[36:39], s72 offen offset:0 lds\n" \
  "buffer_load_dwordx4 v129, s[36:39], s72 offen offset:1024 lds\n" \
  "buffer_load_dwordx4 v130, s[36:39], s72 offen offset:2048 lds\n" \
  "s_waitcnt vmcnt(16)\n" \
  "s_barrier\n" \
  "ds_read_b128 v[176:179], v144 offset:0\n" \
  "ds_read_b128 v[64:67], v146 offset:0\n" \
  "ds_read_b128 v[68:71], v146 offset:2048\n" \
  "ds_read_b128 v[72:75], v146 offset:4096\n" \
  "ds_read_b128 v[76:79], v146 offset:6144\n" \
  "ds_read_b128 v[180:183], v144 offset:2048\n" \
  "s_waitcnt lgkmcnt(3)\n" \
  "v_mfma_f32_16x16x32_bf16 v[0:3], v[176:179], v[64:67], 0\n" \
  "ds_read_b128 v[184:187], v145 offset:0\n" \
  "s_add_u32 s61, s80, 1\n" \
  "s_and_b32 s61, s61, 1\n" \
  "v_mfma_f32_16x16x32_bf16 v[4:7], v[176:179], v[68:71], 0\n" \
  "ds_read_b128 v[80:83], v147 offset:0\n" \
  "s_lshl_b32 s83, s61, 1\n" \
  "s_waitcnt lgkmcnt(2)\n" \
  "v_mfma_f32_16x16x32_bf16 v[8:11], v[176:179], v[72:75], 0\n" \
  "ds_read_b128 v[84:87], v147 offset:2048\n" \
  "buffer_load_dwordx4 v131, s[36:39], s72 offen offset:3072 lds\n" \
  "v_mfma_f32_16x16x32_bf16 v[12:15], v[176:179], v[76:79], 0\n" \
  "ds_read_b128 v[88:91], v147 offset:4096\n" \
  "s_add_u32 m0, s73, 90112\n" \
  "v_mfma_f32_16x16x32_bf16 v[28:31], v[180:183], v[76:79], 0\n" \
  "ds_read_b128 v[92:95], v147 offset:6144\n" \
  "s_cmp_eq_u32 s80, 0\n" \
  "s_cselect_b32 s62, 3, 0\n" \
  "v_mfma_f32_16x16x32_bf16 v[24:27], v[180:183], v[72:75], 0\n" \
  "ds_read_b128 v[188:191], v145 offset:2048\n" \
  "buffer_load_dwordx4 v132, s[44:47], s72 offen offset:0 lds\n" \
  "s_xor_b32 s62, s62, s81\n" \
  "v_mfma_f32_16x16x32_bf16 v[20:23], v[180:183], v[68:71], 0\n" \
  "s_lshl_b32 s62, s62, 1\n" \
  "s_add_u32 s62, s62, s61\n" \
  "s_lshl_b32 s82, s62, 6\n" \
  "v_mfma_f32_16x16x32_bf16 v[16:19], v[180:183], v[64:67], 0\n" \
  "buffer_load_dwordx4 v133, s[44:47], s72 offen offset:1024 lds\n" \
  "s_waitcnt vmcnt(13) lgkmcnt(0)\n" \
  "s_barrier\n" \
  "v_mfma_f32_16x16x32_bf16 v[0:3], v[184:187], v[80:83], v[0:3]\n" \
  "ds_read_b128 v[192:195], v144 offset:16384\n" \
  "s_add_u32 m0, s68, 0\n" \
  "s_add_u32 s72, s72, 128\n" \
  "v_mfma_f32_16x16x32_bf16 v[4:7], v[184:187], v[84:87], v[4:7]\n" \
  "ds_read_b128 v[96:99], v146 offset:8192\n" \
  "s_cmpk_eq_u32 s72, 1280\n" \
  "s_cselect_b32 s72, 0, s72\n" \
  "v_mfma_f32_16x16x32_bf16 v[8:11], v[184:187], v[88:91], v[8:11]\n" \
  "ds_read_b128 v[100:103], v146 offset:10240\n" \
  "buffer_load_dwordx4 v128, s[36:39], s72 offen offset:0 lds\n" \
  "s_sub_u32 s61, s80, 1\n" \
  "v_mfma_f32_16x16x32_bf16 v[12:15], v[184:187], v[92:95], v[12:15]\n" \
  "ds_read_b128 v[104:107], v146 offset:12288\n" \
  "s_cmp_lt_u32 s61, 2\n" \
  "s_cselect_b64 s[84:85], -1, 0\n" \
  "v_mfma_f32_16x16x32_bf16 v[28:31], v[188:191], v[92:95], v[28:31]\n" \
  "ds_read_b128 v[108:111], v146 offset:14336\n" \
  "s_lshr_b32 s61, s80, 1\n" \
  "s_add_u32 s61, s61, 1\n" \
  "v_mfma_f32_16x16x32_bf16 v[24:27], v[188:191], v[88:91], v[24:27]\n" \
  "ds_read_b128 v[196:199], v144 offset:18432\n" \
  "buffer_load_dwordx4 v129, s[36:39], s72 offen offset:1024 lds\n" \
  "s_xor_b32 s61, s61, s81\n" \
  "v_mfma_f32_16x16x32_bf16 v[20:23], v[188:191], v[84:87], v[20:23]\n" \
  "s_lshl_b32 s61, s61, 1\n" \
  "s_and_b32 s62, s80, 1\n" \
  "s_add_u32 s61, s61, s62\n" \
  "v_mfma_f32_16x16x32_bf16 v[16:19], v[188:191], v[80:83], v[16:19]\n" \
  "buffer_load_dwordx4 v130, s[36:39], s72 offen offset:2048 lds\n" \
  "s_lshl_b32 s75, s61, 6\n" \
  "s_waitcnt lgkmcnt(3)\n" \
  "v_mfma_f32_16x16x32_bf16 v[0:3], v[192:195], v[96:99], v[0:3]\n" \
  "ds_read_b128 v[200:203], v145 offset:16384\n" \
  "s_sub_u32 s61, s82, s75\n" \
  "s_mul_i32 s61, s61, 1280\n" \
  "v_mfma_f32_16x16x32_bf16 v[4:7], v[192:195], v[100:103], v[4:7]\n" \
  "ds_read_b128 v[112:115], v147 offset:8192\n" \
  "v_add_u32 v134, s61, v132\n" \
  "s_waitcnt lgkmcnt(2)\n" \
  "v_mfma_f32_16x16x32_bf16 v[8:11], v[192:195], v[104:107], v[8:11]\n" \
  "ds_read_b128 v[116:119], v147 offset:10240\n" \
  "buffer_load_dwordx4 v131, s[36:39], s72 offen offset:3072 lds\n" \
  "v_add_u32 v135, s61, v133\n" \
  "v_mfma_f32_16x16x32_bf16 v[12:15], v[192:195], v[108:111], v[12:15]\n" \
  "ds_read_b128 v[120:123], v147 offset:12288\n" \
  "s_add_u32 m0, s73, 65536\n" \
  "s_and_b32 s61, s79, 1\n" \
  "v_mfma_f32_16x16x32_bf16 v[28:31], v[196:199], v[108:111], v[28:31]\n" \
  "ds_read_b128 v[124:127], v147 offset:14336\n" \
  "s_lshl_b32 s61, s61, 2\n" \
  "s_lshr_b32 s62, s79, 1\n" \
  "v_mfma_f32_16x16x32_bf16 v[24:27], v[196:199], v[104:107], v[24:27]\n" \
  "ds_read_b128 v[204:207], v145 offset:18432\n" \
  "buffer_load_dwordx4 v132, s[44:47], s72 offen offset:0 lds\n" \
  "s_add_u32 s61, s61, s62\n" \
  "v_mfma_f32_16x16x32_bf16 v[20:23], v[196:199], v[100:103], v[20:23]\n" \
  "s_add_u32 s61, s61, s83\n" \
  "s_add_u32 s61, s61, 32\n" \
  "s_mul_i32 s61, s61, 20480\n" \
  "v_mfma_f32_16x16x32_bf16 v[16:19], v[196:199], v[96:99], v[16:19]\n" \
  "buffer_load_dwordx4 v133, s[44:47], s72 offen offset:1024 lds\n" \
  "s_waitcnt vmcnt(12) lgkmcnt(0)\n" \
  "s_barrier\n" \
  "v_mfma_f32_16x16x32_bf16 v[0:3], v[200:203], v[112:115], v[0:3]\n" \
  "ds_read_b128 v[208:211], v144 offset:32768\n" \
  "s_add_u32 m0, s68, 16384\n" \
  "s_add_u32 s72, s72, 128\n" \
  "v_mfma_f32_16x16x32_bf16 v[4:7], v[200:203], v[116:119], v[4:7]\n" \
  "ds_read_b128 v[64:67], v146 offset:16384\n" \
  "s_cmpk_eq_u32 s72, 1280\n" \
  "s_cselect_b32 s72, 0, s72\n" \
  "v_mfma_f32_16x16x32_bf16 v[8:11], v[200:203], v[120:123], v[8:11]\n" \
  "ds_read_b128 v[68:71], v146 offset:18432\n" \
  "buffer_load_dwordx4 v128, s[36:39], s72 offen offset:0 nt lds\n" \
  "v_bfe_u32 v56, %[tid], 3, 3\n" \
  "v_mfma_f32_16x16x32_bf16 v[12:15], v[200:203], v[124:127], v[12:15]\n" \
  "ds_read_b128 v[72:75], v146 offset:20480\n" \
  "v_mul_u32_u24 v56, 1280, v56\n" \
  "v_and_b32 v57, 0x70, v132\n" \
  "v_mfma_f32_16x16x32_bf16 v[28:31], v[204:207], v[124:127], v[28:31]\n" \
  "ds_read_b128 v[76:79], v146 offset:22528\n" \
  "v_add3_u32 v56, v56, s61, v57\n" \
  "v_xor_b32 v57, 64, v56\n" \
  "v_mfma_f32_16x16x32_bf16 v[24:27], v[204:207], v[120:123], v[24:27]\n" \
  "ds_read_b128 v[212:215], v144 offset:34816\n" \
  "buffer_load_dwordx4 v129, s[36:39], s72 offen offset:1024 nt lds\n" \
  "v_add_u32 v57, 9216, v57\n" \
  "v_mfma_f32_16x16x32_bf16 v[20:23], v[204:207], v[116:119], v[20:23]\n" \
  "v_cndmask_b32 v134, v134, v56, s[84:85]\n" \
  "v_cndmask_b32 v135, v135, v57, s[84:85]\n" \
  "v_and_b32 v56, 35, %[tid]\n" \
  "v_mfma_f32_16x16x32_bf16 v[16:19], v[204:207], v[112:115], v[16:19]\n" \
  "buffer_load_dwordx4 v130, s[36:39], s72 offen offset:2048 nt lds\n" \
  "v_bfe_u32 v57, %[tid], 4, 1\n" \
  "s_waitcnt lgkmcnt(3)\n" \
  "v_mfma_f32_16x16x32_bf16 v[0:3], v[208:211], v[64:67], v[0:3]\n" \
  "ds_read_b128 v[216:219], v145 offset:32768\n" \
  "v_lshl_or_b32 v56, v57, 2, v56\n" \
  "v_bfe_u32 v58, %[tid], 2, 2\n" \
  "v_mfma_f32_16x16x32_bf16 v[4:7], v[208:211], v[68:71], v[4:7]\n" \
  "ds_read_b128 v[80:83], v147 offset:16384\n" \
  "v_lshl_or_b32 v56, v58, 3, v56\n" \
  "s_waitcnt lgkmcnt(2)\n" \
  "v_mfma_f32_16x16x32_bf16 v[8:11], v[208:211], v[72:75], v[8:11]\n" \
  "ds_read_b128 v[84:87], v147 offset:18432\n" \
  "buffer_load_dwordx4 v131, s[36:39], s72 offen offset:3072 nt lds\n" \
  "v_and_b32 v58, 15, %[tid]\n" \
  "v_mfma_f32_16x16x32_bf16 v[12:15], v[208:211], v[76:79], v[12:15]\n" \
  "ds_read_b128 v[88:91], v147 offset:20480\n" \
  "s_add_u32 m0, s73, 73728\n" \
  "v_lshl_or_b32 v57, v57, 6, v58\n" \
  "v_mfma_f32_16x16x32_bf16 v[28:31], v[212:215], v[76:79], v[28:31]\n" \
  "ds_read_b128 v[92:95], v147 offset:22528\n" \
  "v_bfe_u32 v58, %[tid], 5, 1\n" \
  "v_lshl_or_b32 v57, v58, 4, v57\n" \
  "v_mfma_f32_16x16x32_bf16 v[24:27], v[212:215], v[72:75], v[24:27]\n" \
  "ds_read_b128 v[220:223], v145 offset:34816\n" \
  "buffer_load_dwordx4 v132, s[44:47], s72 offen offset:0 nt lds\n" \
  "s_lshr_b32 s61, s81, 1\n" \
  "v_mfma_f32_16x16x32_bf16 v[20:23], v[212:215], v[68:71], v[20:23]\n" \
  "s_mul_i32 s61, s61, 640\n" \
  "s_add_u32 s61, s61, 4096\n" \
  "s_add_u32 s62, s61, s75\n" \
  "v_mfma_f32_16x16x32_bf16 v[16:19], v[212:215], v[64:67], v[16:19]\n" \
  "buffer_load_dwordx4 v133, s[44:47], s72 offen offset:1024 nt lds\n" \
  "s_waitcnt vmcnt(12) lgkmcnt(0)\n" \
  "s_barrier\n" \
  "v_mfma_f32_16x16x32_bf16 v[0:3], v[216:219], v[80:83], v[0:3]\n" \
  "ds_read_b128 v[224:227], v144 offset:49152\n" \
  "s_add_u32 m0, s68, 32768\n" \
  "s_add_u32 s72, s72, 128\n" \
  "v_mfma_f32_16x16x32_bf16 v[4:7], v[216:219], v[84:87], v[4:7]\n" \
  "ds_read_b128 v[96:99], v146 offset:24576\n" \
  "s_cmpk_eq_u32 s72, 1280\n" \
  "s_cselect_b32 s72, 0, s72\n" \
  "v_mfma_f32_16x16x32_bf16 v[8:11], v[216:219], v[88:91], v[8:11]\n" \
  "ds_read_b128 v[100:103], v146 offset:26624\n" \
  "buffer_load_dwordx4 v128, s[36:39], s72 offen offset:0 nt lds\n" \
  "v_add_lshl_u32 v149, v56, s62, 3\n" \
  "v_mfma_f32_16x16x32_bf16 v[12:15], v[216:219], v[92:95], v[12:15]\n" \
  "ds_read_b128 v[104:107], v146 offset:28672\n" \
  "s_add_u32 s62, s61, s82\n" \
  "v_add_lshl_u32 v150, v56, s62, 3\n" \
  "v_mfma_f32_16x16x32_bf16 v[28:31], v[220:223], v[92:95], v[28:31]\n" \
  "ds_read_b128 v[108:111], v146 offset:30720\n" \
  "s_lshl_b32 s62, s83, 4\n" \
  "s_add_u32 s62, s62, s61\n" \
  "v_mfma_f32_16x16x32_bf16 v[24:27], v[220:223], v[88:91], v[24:27]\n" \
  "ds_read_b128 v[228:231], v144 offset:51200\n" \
  "buffer_load_dwordx4 v129, s[36:39], s72 offen offset:1024 nt lds\n" \
  "s_add_u32 s62, s62, 512\n" \
  "v_mfma_f32_16x16x32_bf16 v[20:23], v[220:223], v[84:87], v[20:23]\n" \
  "v_add_lshl_u32 v57, v57, s62, 3\n" \
  "v_cndmask_b32 v150, v150, v57, s[84:85]\n" \
  "s_and_b32 s61, %[bid], 3\n" \
  "v_mfma_f32_16x16x32_bf16 v[16:19], v[220:223], v[80:83], v[16:19]\n" \
  "buffer_load_dwordx4 v130, s[36:39], s72 offen offset:2048 nt lds\n" \
  "s_lshl_b32 s61, s61, 2\n" \
  "s_waitcnt lgkmcnt(3)\n" \
  "v_mfma_f32_16x16x32_bf16 v[0:3], v[224:227], v[96:99], v[0:3]\n" \
  "ds_read_b128 v[232:235], v145 offset:49152\n" \
  "s_add_u32 s61, s61, s78\n" \
  "s_lshl_b32 s62, s61, 2\n" \
  "v_mfma_f32_16x16x32_bf16 v[4:7], v[224:227], v[100:103], v[4:7]\n" \
  "ds_read_b128 v[112:115], v147 offset:24576\n" \
  "s_add_u32 s62, s62, s79\n" \
  "s_waitcnt lgkmcnt(2)\n" \
  "v_mfma_f32_16x16x32_bf16 v[8:11], v[224:227], v[104:107], v[8:11]\n" \
  "ds_read_b128 v[116:119], v147 offset:26624\n" \
  "buffer_load_dwordx4 v131, s[36:39], s72 offen offset:3072 nt lds\n" \
  "s_lshl_b32 s63, s62, 5\n" \
  "v_mfma_f32_16x16x32_bf16 v[12:15], v[224:227], v[108:111], v[12:15]\n" \
  "ds_read_b128 v[120:123], v147 offset:28672\n" \
  "s_add_u32 m0, s73, 81920\n" \
  "s_add_u32 s63, s63, 2048\n" \
  "v_mfma_f32_16x16x32_bf16 v[28:31], v[228:231], v[108:111], v[28:31]\n" \
  "ds_read_b128 v[124:127], v147 offset:30720\n" \
  "v_and_b32 v56, 31, %[tid]\n" \
  "v_add_lshl_u32 v148, v56, s63, 3\n" \
  "v_mfma_f32_16x16x32_bf16 v[24:27], v[228:231], v[104:107], v[24:27]\n" \
  "ds_read_b128 v[236:239], v145 offset:51200\n" \
  "buffer_load_dwordx4 v132, s[44:47], s72 offen offset:0 nt lds\n" \
  "s_and_b32 s62, s62, 31\n" \
  "v_mfma_f32_16x16x32_bf16 v[20:23], v[228:231], v[100:103], v[20:23]\n" \
  "PAD0_%=:\n" \
  "s_cbranch_execz PAD4_%=\n" \
  "s_mul_i32 s63, s81, 10\n" \
  "s_add_u32 s63, s63, s80\n" \
  "s_lshl_b32 s86, s63, 6\n" \
  "v_mfma_f32_16x16x32_bf16 v[16:19], v[228:231], v[96:99], v[16:19]\n" \
  "buffer_load_dwordx4 v133, s[44:47], s72 offen offset:1024 nt lds\n" \
  "s_waitcnt vmcnt(12) lgkmcnt(0)\n" \
  "s_barrier\n" \
  "v_mfma_f32_16x16x32_bf16 v[0:3], v[232:235], v[112:115], v[0:3]\n" \
  "ds_read_b128 a[160:163], v144 offset:0\n" \
  "s_add_u32 m0, s68, 49152\n" \
  "s_add_u32 s72, s72, 128\n" \
  "v_mfma_f32_16x16x32_bf16 v[4:7], v[232:235], v[116:119], v[4:7]\n" \
  "ds_read_b128 v[64:67], v146 offset:0\n" \
  "s_cmpk_eq_u32 s72, 1280\n" \
  "s_cselect_b32 s72, 0, s72\n" \
  "v_mfma_f32_16x16x32_bf16 v[8:11], v[232:235], v[120:123], v[8:11]\n" \
  "ds_read_b128 v[68:71], v146 offset:2048\n" \
  "buffer_load_dwordx4 v128, s[36:39], s72 offen offset:0 nt lds\n" \
  "s_add_u32 s62, s62, s86\n" \
  "v_mfma_f32_16x16x32_bf16 v[12:15], v[232:235], v[124:127], v[12:15]\n" \
  "ds_read_b128 v[72:75], v146 offset:4096\n" \
  "s_lshl_b32 s62, s62, 7\n" \
  "s_add_u32 s62, s62, 65536\n" \
  "v_mfma_f32_16x16x32_bf16 v[28:31], v[236:239], v[124:127], v[28:31]\n" \
  "ds_read_b128 v[76:79], v146 offset:6144\n" \
  "v_and_b32 v56, 63, %[tid]\n" \
  "v_add_lshl_u32 v151, v56, s62, 4\n" \
  "v_mfma_f32_16x16x32_bf16 v[24:27], v[236:239], v[120:123], v[24:27]\n" \
  "ds_read_b128 a[164:167], v144 offset:2048\n" \
  "buffer_load_dwordx4 v129, s[36:39], s72 offen offset:1024 nt lds\n" \
  "s_and_b32 s61, s61, 7\n" \
  "v_mfma_f32_16x16x32_bf16 v[20:23], v[236:239], v[116:119], v[20:23]\n" \
  "s_lshl_b32 s63, s63, 3\n" \
  "s_add_u32 s61, s61, s63\n" \
  "s_lshl_b32 s61, s61, 7\n" \
  "v_mfma_f32_16x16x32_bf16 v[16:19], v[236:239], v[112:115], v[16:19]\n" \
  "buffer_load_dwordx4 v130, s[36:39], s72 offen offset:2048 nt lds\n" \
  "s_add_u32 s61, s61, 49152\n" \
  "s_waitcnt lgkmcnt(3)\n" \
  "v_mfma_f32_16x16x32_bf16 v[0:3], a[160:163], v[64:67], v[0:3]\n" \
  "ds_read_b128 a[168:171], v145 offset:0\n" \
  "s_cmp_eq_u32 s79, 0\n" \
  "s_cselect_b32 s86, s61, 33554432\n" \
  "v_mfma_f32_16x16x32_bf16 v[4:7], a[160:163], v[68:71], v[4:7]\n" \
  "ds_read_b128 v[80:83], v147 offset:0\n" \
  "s_add_u32 s69, s86, 5120\n" \
  "s_waitcnt lgkmcnt(2)\n" \
  "v_mfma_f32_16x16x32_bf16 v[8:11], a[160:163], v[72:75], v[8:11]\n" \
  "ds_read_b128 v[84:87], v147 offset:2048\n" \
  "buffer_load_dwordx4 v131, s[36:39], s72 offen offset:3072 nt lds\n" \
  "s_add_u32 s61, s68, 147456\n" \
  "v_mfma_f32_16x16x32_bf16 v[12:15], a[160:163], v[76:79], v[12:15]\n" \
  "ds_read_b128 v[88:91], v147 offset:4096\n" \
  "s_add_u32 m0, s73, 90112\n" \
  "v_lshl_add_u32 v152, v56, 2, s61\n" \
  "v_mfma_f32_16x16x32_bf16 v[28:31], a[164:167], v[76:79], v[28:31]\n" \
  "ds_read_b128 v[92:95], v147 offset:6144\n" \
  "v_bfe_u32 v57, %[tid], 4, 2\n" \
  "v_lshl_add_u32 v153, v57, 5, s61\n" \
  "v_mfma_f32_16x16x32_bf16 v[24:27], a[164:167], v[72:75], v[24:27]\n" \
  "ds_read_b128 a[172:175], v145 offset:2048\n" \
  "buffer_load_dwordx4 v132, s[44:47], s72 offen offset:0 nt lds\n" \
  "v_and_b32 v57, 15, %[tid]\n" \
  "v_mfma_f32_16x16x32_bf16 v[20:23], a[164:167], v[68:71], v[20:23]\n" \
  "v_lshl_add_u32 v154, v57, 2, s61\n" \
  "s_lshl_b32 s87, s81, 30\n" \
  "s_and_b32 s87, s87, 0x80000000\n" \
  "v_mfma_f32_16x16x32_bf16 v[16:19], a[164:167], v[64:67], v[16:19]\n" \
  "buffer_load_dwordx4 v133, s[44:47], s72 offen offset:1024 nt lds\n" \
  "s_waitcnt vmcnt(12) lgkmcnt(0)\n" \
  "s_barrier\n" \
  "v_mfma_f32_16x16x32_bf16 v[0:3], a[168:171], v[80:83], v[0:3]\n" \
  "ds_read_b128 a[176:179], v144 offset:16384\n" \
  "s_add_u32 m0, s68, 0\n" \
  "s_add_u32 s72, s72, 128\n" \
  "v_mfma_f32_16x16x32_bf16 v[4:7], a[168:171], v[84:87], v[4:7]\n" \
  "ds_read_b128 v[96:99], v146 offset:8192\n" \
  "s_cmpk_eq_u32 s72, 1280\n" \
  "s_cselect_b32 s72, 0, s72\n" \
  "v_mfma_f32_16x16x32_bf16 v[8:11], a[168:171], v[88:91], v[8:11]\n" \
  "ds_read_b128 v[100:103], v146 offset:10240\n" \
  "buffer_load_dwordx4 v128, s[36:39], s72 offen offset:0 nt lds\n" \
  "v_mov_b32 v166, s71\n" \
  "v_mfma_f32_16x16x32_bf16 v[12:15], a[168:171], v[92:95], v[12:15]\n" \
  "ds_read_b128 v[104:107], v146 offset:12288\n" \
  "v_and_b32 v57, 32, %[tid]\n" \
  "v_lshlrev_b32 v57, 20, v57\n" \
  "v_mfma_f32_16x16x32_bf16 v[28:31], a[172:175], v[92:95], v[28:31]\n" \
  "ds_read_b128 v[108:111], v146 offset:14336\n" \
  "v_lshl_or_b32 v167, v56, 2, v57\n" \
  "v_mfma_f32_16x16x32_bf16 v[24:27], a[172:175], v[88:91], v[24:27]\n" \
  "ds_read_b128 a[180:183], v144 offset:18432\n" \
  "buffer_load_dwordx4 v129, s[36:39], s72 offen offset:1024 nt lds\n" \
  "v_mfma_f32_16x16x32_bf16 v[20:23], a[172:175], v[84:87], v[20:23]\n" \
  "v_mfma_f32_16x16x32_bf16 v[16:19], a[172:175], v[80:83], v[16:19]\n" \
  "buffer_load_dwordx4 v130, s[36:39], s72 offen offset:2048 nt lds\n" \
  "s_waitcnt lgkmcnt(3)\n" \
  "v_mfma_f32_16x16x32_bf16 v[0:3], a[176:179], v[96:99], v[0:3]\n" \
  "ds_read_b128 a[184:187], v145 offset:16384\n" \
  "v_mfma_f32_16x16x32_bf16 v[4:7], a[176:179], v[100:103], v[4:7]\n" \
  "ds_read_b128 v[112:115], v147 offset:8192\n" \
  "s_waitcnt lgkmcnt(2)\n" \
  "v_mfma_f32_16x16x32_bf16 v[8:11], a[176:179], v[104:107], v[8:11]\n" \
  "ds_read_b128 v[116:119], v147 offset:10240\n" \
  "buffer_load_dwordx2 v[160:161], v148, s[40:43], 0 offen sc1\n" \
  "v_mfma_f32_16x16x32_bf16 v[12:15], a[176:179], v[108:111], v[12:15]\n" \
  "ds_read_b128 v[120:123], v147 offset:12288\n" \
  "v_mfma_f32_16x16x32_bf16 v[28:31], a[180:183], v[108:111], v[28:31]\n" \
  "ds_read_b128 v[124:127], v147 offset:14336\n" \
  "v_mfma_f32_16x16x32_bf16 v[24:27], a[180:183], v[104:107], v[24:27]\n" \
  "ds_read_b128 a[188:191], v145 offset:18432\n" \
  "buffer_load_dwordx2 v[162:163], v149, s[40:43], 0 offen sc1\n" \
  "v_mfma_f32_16x16x32_bf16 v[20:23], a[180:183], v[100:103], v[20:23]\n" \
  "v_mfma_f32_16x16x32_bf16 v[16:19], a[180:183], v[96:99], v[16:19]\n" \
  "buffer_load_dwordx2 v[164:165], v150, s[40:43], 0 offen sc1\n" \
  "s_waitcnt vmcnt(12) lgkmcnt(0)\n" \
  "s_barrier\n" \
  "v_mfma_f32_16x16x32_bf16 v[0:3], a[184:187], v[112:115], v[0:3]\n" \
  "ds_read_b128 a[192:195], v144 offset:32768\n" \
  "v_mfma_f32_16x16x32_bf16 v[4:7], a[184:187], v[116:119], v[4:7]\n" \
  "ds_read_b128 v[64:67], v146 offset:16384\n" \
  "v_mfma_f32_16x16x32_bf16 v[8:11], a[184:187], v[120:123], v[8:11]\n" \
  "ds_read_b128 v[68:71], v146 offset:18432\n" \
  "buffer_load_dwordx4 v131, s[36:39], s72 offen offset:3072 nt lds\n" \
  "v_mfma_f32_16x16x32_bf16 v[12:15], a[184:187], v[124:127], v[12:15]\n" \
  "ds_read_b128 v[72:75], v146 offset:20480\n" \
  "s_add_u32 m0, s73, 65536\n" \
  "v_mfma_f32_16x16x32_bf16 v[28:31], a[188:191], v[124:127], v[28:31]\n" \
  "ds_read_b128 v[76:79], v146 offset:22528\n" \
  "v_mfma_f32_16x16x32_bf16 v[24:27], a[188:191], v[120:123], v[24:27]\n" \
  "ds_read_b128 a[196:199], v144 offset:34816\n" \
  "buffer_load_dwordx4 v132, s[44:47], s72 offen offset:0 nt lds\n" \
  "v_mfma_f32_16x16x32_bf16 v[20:23], a[188:191], v[116:119], v[20:23]\n" \
  "v_mfma_f32_16x16x32_bf16 v[16:19], a[188:191], v[112:115], v[16:19]\n" \
  "buffer_load_dwordx4 v133, s[44:47], s72 offen offset:1024 nt lds\n" \
  "s_waitcnt lgkmcnt(3)\n" \
  "v_mfma_f32_16x16x32_bf16 v[0:3], a[192:195], v[64:67], v[0:3]\n" \
  "ds_read_b128 a[200:203], v145 offset:32768\n" \
  "s_add_u32 m0, s68, 16384\n" \
  "s_add_u32 s72, s72, 128\n" \
  "v_mfma_f32_16x16x32_bf16 v[4:7], a[192:195], v[68:71], v[4:7]\n" \
  "ds_read_b128 v[80:83], v147 offset:16384\n" \
  "s_cmpk_eq_u32 s72, 1280\n" \
  "s_cselect_b32 s72, 0, s72\n" \
  "s_waitcnt lgkmcnt(2)\n" \
  "v_mfma_f32_16x16x32_bf16 v[8:11], a[192:195], v[72:75], v[8:11]\n" \
  "ds_read_b128 v[84:87], v147 offset:18432\n" \
  "buffer_load_dwordx4 v128, s[36:39], s72 offen offset:0 nt lds\n" \
  "v_mfma_f32_16x16x32_bf16 v[12:15], a[192:195], v[76:79], v[12:15]\n" \
  "ds_read_b128 v[88:91], v147 offset:20480\n" \
  "v_mfma_f32_16x16x32_bf16 v[28:31], a[196:199], v[76:79], v[28:31]\n" \
  "ds_read_b128 v[92:95], v147 offset:22528\n" \
  "v_mfma_f32_16x16x32_bf16 v[24:27], a[196:199], v[72:75], v[24:27]\n" \
  "ds_read_b128 a[204:207], v145 offset:34816\n" \
  "buffer_load_dwordx4 v129, s[36:39], s72 offen offset:1024 nt lds\n" \
  "v_mfma_f32_16x16x32_bf16 v[20:23], a[196:199], v[68:71], v[20:23]\n" \
  "v_mfma_f32_16x16x32_bf16 v[16:19], a[196:199], v[64:67], v[16:19]\n" \
  "buffer_load_dwordx4 v130, s[36:39], s72 offen offset:2048 nt lds\n" \
  "s_waitcnt vmcnt(6) lgkmcnt(3)\n" \
  "s_barrier\n" \
  "v_mfma_f32_16x16x32_bf16 v[0:3], a[200:203], v[80:83], v[0:3]\n" \
  "ds_read_b128 a[208:211], v144 offset:49152\n" \
  "v_xor_b32 v56, s71, v161\n" \
  "v_mfma_f32_16x16x32_bf16 v[4:7], a[200:203], v[84:87], v[4:7]\n" \
  "ds_read_b128 v[96:99], v146 offset:24576\n" \
  "v_bitop3_b32 v56, v163, v56, s71 bitop3:0xde\n" \
  "s_waitcnt lgkmcnt(2)\n" \
  "v_mfma_f32_16x16x32_bf16 v[8:11], a[200:203], v[88:91], v[8:11]\n" \
  "ds_read_b128 v[100:103], v146 offset:26624\n" \
  "buffer_load_dwordx4 v131, s[36:39], s72 offen offset:3072 nt lds\n" \
  "v_bitop3_b32 v56, v165, v56, s71 bitop3:0xde\n" \
  "v_mfma_f32_16x16x32_bf16 v[12:15], a[200:203], v[92:95], v[12:15]\n" \
  "ds_read_b128 v[104:107], v146 offset:28672\n" \
  "s_add_u32 m0, s73, 73728\n" \
  "v_mfma_f32_16x16x32_bf16 v[28:31], a[204:207], v[92:95], v[28:31]\n" \
  "ds_read_b128 v[108:111], v146 offset:30720\n" \
  "v_cmp_ne_u32 vcc, 0, v56\n" \
  "s_cbranch_vccnz GSPIN_%=\n" \
  "GOK_%=:\n" \
  "v_mfma_f32_16x16x32_bf16 v[24:27], a[204:207], v[88:91], v[24:27]\n" \
  "ds_read_b128 a[212:215], v144 offset:51200\n" \
  "buffer_load_dwordx4 v132, s[44:47], s72 offen offset:0 nt lds\n" \
  "v_and_b32 v56, 0xffff0000, v160\n" \
  "v_mfma_f32_16x16x32_bf16 v[20:23], a[204:207], v[84:87], v[20:23]\n" \
  "v_rcp_f32 v56, v56\n" \
  "v_mfma_f32_16x16x32_bf16 v[16:19], a[204:207], v[80:83], v[16:19]\n" \
  "buffer_load_dwordx4 v133, s[44:47], s72 offen offset:1024 nt lds\n" \
  "v_lshlrev_b32 v57, 16, v160\n" \
  "s_waitcnt lgkmcnt(3)\n" \
  "v_mfma_f32_16x16x32_bf16 v[0:3], a[208:211], v[96:99], v[0:3]\n" \
  "ds_read_b128 a[216:219], v145 offset:49152\n" \
  "s_add_u32 m0, s68, 32768\n" \
  "s_add_u32 s72, s72, 128\n" \
  "v_mfma_f32_16x16x32_bf16 v[4:7], a[208:211], v[100:103], v[4:7]\n" \
  "ds_read_b128 v[112:115], v147 offset:24576\n" \
  "s_cmpk_eq_u32 s72, 1280\n" \
  "s_cselect_b32 s72, 0, s72\n" \
  "s_waitcnt lgkmcnt(2)\n" \
  "v_mfma_f32_16x16x32_bf16 v[8:11], a[208:211], v[104:107], v[8:11]\n" \
  "ds_read_b128 v[116:119], v147 offset:26624\n" \
  "buffer_load_dwordx4 v134, s[44:47], s72 offen offset:0 lds\n" \
  "v_xor_b32 v57, 0x80000000, v57\n" \
  "v_mfma_f32_16x16x32_bf16 v[12:15], a[208:211], v[108:111], v[12:15]\n" \
  "ds_read_b128 v[120:123], v147 offset:28672\n" \
  "buffer_load_dwordx4 v135, s[44:47], s72 offen offset:1024 lds\n" \
  "v_xor_b32 v56, s87, v56\n" \
  "v_mfma_f32_16x16x32_bf16 v[28:31], a[212:215], v[108:111], v[28:31]\n" \
  "ds_read_b128 v[124:127], v147 offset:30720\n" \
  "s_add_u32 m0, s68, 34816\n" \
  "s_add_u32 s72, s72, 128\n" \
  "v_mfma_f32_16x16x32_bf16 v[24:27], a[212:215], v[104:107], v[24:27]\n" \
  "ds_read_b128 a[220:223], v145 offset:51200\n" \
  "s_cmpk_eq_u32 s72, 1280\n" \
  "s_cselect_b32 s72, 0, s72\n" \
  "v_mfma_f32_16x16x32_bf16 v[20:23], a[212:215], v[100:103], v[20:23]\n" \
  "buffer_load_dwordx4 v134, s[44:47], s72 offen offset:0 lds\n" \
  "ds_write2st64_b32 v152, v56, v57 offset1:1\n" \
  "v_mfma_f32_16x16x32_bf16 v[16:19], a[212:215], v[96:99], v[16:19]\n" \
  "buffer_load_dwordx4 v135, s[44:47], s72 offen offset:1024 lds\n" \
  "s_waitcnt vmcnt(10) lgkmcnt(4)\n" \
  "s_barrier\n" \
  "v_mfma_f32_16x16x32_bf16 v[0:3], a[216:219], v[112:115], v[0:3]\n" \
  "ds_read_b128 a[224:227], v144 offset:0\n" \
  "s_add_u32 m0, s73, 81920\n" \
  "ds_write2st64_b32 v152, v162, v164 offset0:2 offset1:3\n" \
  "v_mfma_f32_16x16x32_bf16 v[4:7], a[216:219], v[116:119], v[4:7]\n" \
  "ds_read_b128 v[64:67], v146 offset:0\n" \
  "s_add_u32 s72, s72, 128\n" \
  "s_waitcnt lgkmcnt(4)\n" \
  "v_mfma_f32_16x16x32_bf16 v[8:11], a[216:219], v[120:123], v[8:11]\n" \
  "ds_read_b128 v[68:71], v146 offset:2048\n" \
  "s_cmpk_eq_u32 s72, 1280\n" \
  "ds_read_b128 v[32:35], v153\n" \
  "v_mfma_f32_16x16x32_bf16 v[12:15], a[216:219], v[124:127], v[12:15]\n" \
  "ds_read_b128 v[72:75], v146 offset:4096\n" \
  "s_cselect_b32 s72, 0, s72\n" \
  "ds_read_b128 v[36:39], v153 offset:16\n" \
  "v_mfma_f32_16x16x32_bf16 v[28:31], a[220:223], v[124:127], v[28:31]\n" \
  "ds_read_b128 v[76:79], v146 offset:6144\n" \
  "buffer_load_dwordx4 v134, s[44:47], s72 offen offset:0 lds\n" \
  "ds_read_b128 v[40:43], v153 offset:256\n" \
  "v_mfma_f32_16x16x32_bf16 v[24:27], a[220:223], v[120:123], v[24:27]\n" \
  "ds_read_b128 a[228:231], v144 offset:2048\n" \
  "ds_read_b128 v[44:47], v153 offset:272\n" \
  "v_mfma_f32_16x16x32_bf16 v[20:23], a[220:223], v[116:119], v[20:23]\n" \
  "buffer_load_dwordx4 v135, s[44:47], s72 offen offset:1024 lds\n" \
  "ds_read2_b32 v[48:49], v154 offset0:128 offset1:144\n" \
  "v_mfma_f32_16x16x32_bf16 v[16:19], a[220:223], v[112:115], v[16:19]\n" \
  "ds_read2_b32 v[50:51], v154 offset0:160 offset1:176\n" \
  "s_waitcnt lgkmcnt(9)\n" \
  "v_mfma_f32_16x16x32_bf16 v[0:3], a[224:227], v[64:67], v[0:3]\n" \
  "ds_read_b128 a[232:235], v145 offset:0\n" \
  "s_add_u32 m0, s68, 49152\n" \
  "s_add_u32 s72, s72, 128\n" \
  "v_mfma_f32_16x16x32_bf16 v[4:7], a[224:227], v[68:71], v[4:7]\n" \
  "ds_read_b128 v[80:83], v147 offset:0\n" \
  "s_cmpk_eq_u32 s72, 1280\n" \
  "s_cselect_b32 s72, 0, s72\n" \
  "s_waitcnt lgkmcnt(5)\n" \
  "v_mfma_f32_16x16x32_bf16 v[8:11], a[224:227], v[72:75], v[8:11]\n" \
  "ds_read_b128 v[84:87], v147 offset:2048\n" \
  "buffer_load_dwordx4 v134, s[44:47], s72 offen offset:0 lds\n" \
  "ds_read2_b32 v[52:53], v154 offset0:192 offset1:208\n" \
  "v_mfma_f32_16x16x32_bf16 v[12:15], a[224:227], v[76:79], v[12:15]\n" \
  "ds_read_b128 v[88:91], v147 offset:4096\n" \
  "buffer_load_dwordx4 v135, s[44:47], s72 offen offset:1024 lds\n" \
  "ds_read2_b32 v[54:55], v154 offset0:224 offset1:240\n" \
  "v_mfma_f32_16x16x32_bf16 v[28:31], a[228:231], v[76:79], v[28:31]\n" \
  "ds_read_b128 v[92:95], v147 offset:6144\n" \
  "s_add_u32 m0, s68, 51200\n" \
  "s_add_u32 s72, s72, 128\n" \
  "v_mfma_f32_16x16x32_bf16 v[24:27], a[228:231], v[72:75], v[24:27]\n" \
  "ds_read_b128 a[236:239], v145 offset:2048\n" \
  "s_cmpk_eq_u32 s72, 1280\n" \
  "s_cselect_b32 s72, 0, s72\n" \
  "v_mfma_f32_16x16x32_bf16 v[20:23], a[228:231], v[68:71], v[20:23]\n" \
  "buffer_load_dwordx4 v134, s[44:47], s72 offen offset:0 lds\n" \
  "v_mfma_f32_16x16x32_bf16 v[16:19], a[228:231], v[64:67], v[16:19]\n" \
  "buffer_load_dwordx4 v135, s[44:47], s72 offen offset:1024 lds\n" \
  "s_waitcnt vmcnt(10) lgkmcnt(5)\n" \
  "s_barrier\n" \
  "v_mfma_f32_16x16x32_bf16 v[0:3], a[232:235], v[80:83], v[0:3]\n" \
  "ds_read_b128 a[240:243], v144 offset:16384\n" \
  "s_add_u32 m0, s73, 90112\n" \
  "v_mfma_f32_16x16x32_bf16 v[4:7], a[232:235], v[84:87], v[4:7]\n" \
  "ds_read_b128 v[96:99], v146 offset:8192\n" \
  "s_add_u32 s72, s72, 128\n" \
  "s_waitcnt lgkmcnt(2)\n" \
  "v_mfma_f32_16x16x32_bf16 v[8:11], a[232:235], v[88:91], v[8:11]\n" \
  "ds_read_b128 v[100:103], v146 offset:10240\n" \
  "s_cmpk_eq_u32 s72, 1280\n" \
  "v_mfma_f32_16x16x32_bf16 v[12:15], a[232:235], v[92:95], v[12:15]\n" \
  "ds_read_b128 v[104:107], v146 offset:12288\n" \
  "s_cselect_b32 s72, 0, s72\n" \
  "v_mfma_f32_16x16x32_bf16 v[28:31], a[236:239], v[92:95], v[28:31]\n" \
  "ds_read_b128 v[108:111], v146 offset:14336\n" \
  "buffer_load_dwordx4 v134, s[44:47], s72 offen offset:0 lds\n" \
  "v_mfma_f32_16x16x32_bf16 v[24:27], a[236:239], v[88:91], v[24:27]\n" \
  "ds_read_b128 a[244:247], v144 offset:18432\n" \
  "v_mfma_f32_16x16x32_bf16 v[20:23], a[236:239], v[84:87], v[20:23]\n" \
  "buffer_load_dwordx4 v135, s[44:47], s72 offen offset:1024 lds\n" \
  "v_mfma_f32_16x16x32_bf16 v[16:19], a[236:239], v[80:83], v[16:19]\n" \
  "s_waitcnt lgkmcnt(3)\n" \
  "v_mfma_f32_16x16x32_bf16 v[0:3], a[240:243], v[96:99], v[0:3]\n" \
  "ds_read_b128 a[248:251], v145 offset:16384\n" \
  "s_add_u32 m0, s68, 0\n" \
  "s_add_u32 s72, s72, 128\n" \
  "v_mfma_f32_16x16x32_bf16 v[4:7], a[240:243], v[100:103], v[4:7]\n" \
  "ds_read_b128 v[112:115], v147 offset:8192\n" \
  "s_cmpk_eq_u32 s72, 1280\n" \
  "s_cselect_b32 s72, 0, s72\n" \
  "s_waitcnt lgkmcnt(2)\n" \
  "v_mfma_f32_16x16x32_bf16 v[8:11], a[240:243], v[104:107], v[8:11]\n" \
  "ds_read_b128 v[116:119], v147 offset:10240\n" \
  "buffer_load_dwordx4 v134, s[44:47], s72 offen offset:0 lds\n" \
  "v_mfma_f32_16x16x32_bf16 v[12:15], a[240:243], v[108:111], v[12:15]\n" \
  "ds_read_b128 v[120:123], v147 offset:12288\n" \
  "buffer_load_dwordx4 v135, s[44:47], s72 offen offset:1024 lds\n" \
  "v_mfma_f32_16x16x32_bf16 v[28:31], a[244:247], v[108:111], v[28:31]\n" \
  "ds_read_b128 v[124:127], v147 offset:14336\n" \
  "s_add_u32 m0, s68, 2048\n" \
  "s_add_u32 s72, s72, 128\n" \
  "v_mfma_f32_16x16x32_bf16 v[24:27], a[244:247], v[104:107], v[24:27]\n" \
  "ds_read_b128 a[252:255], v145 offset:18432\n" \
  "s_cmpk_eq_u32 s72, 1280\n" \
  "s_cselect_b32 s72, 0, s72\n" \
  "v_mfma_f32_16x16x32_bf16 v[20:23], a[244:247], v[100:103], v[20:23]\n" \
  "buffer_load_dwordx4 v134, s[44:47], s72 offen offset:0 lds\n" \
  "v_mfma_f32_16x16x32_bf16 v[16:19], a[244:247], v[96:99], v[16:19]\n" \
  "buffer_load_dwordx4 v135, s[44:47], s72 offen offset:1024 lds\n" \
  "s_waitcnt lgkmcnt(3)\n" \
  "v_mfma_f32_16x16x32_bf16 v[0:3], a[248:251], v[112:115], v[0:3]\n" \
  "v_mfma_f32_16x16x32_bf16 v[4:7], a[248:251], v[116:119], v[4:7]\n" \
  "s_waitcnt lgkmcnt(0)\n" \
  "v_mfma_f32_16x16x32_bf16 v[8:11], a[248:251], v[120:123], v[8:11]\n" \
  "v_mfma_f32_16x16x32_bf16 v[12:15], a[248:251], v[124:127], v[12:15]\n" \
  "v_mfma_f32_16x16x32_bf16 v[28:31], a[252:255], v[124:127], v[28:31]\n" \
  "v_mfma_f32_16x16x32_bf16 v[24:27], a[252:255], v[120:123], v[24:27]\n" \
  "v_mfma_f32_16x16x32_bf16 v[20:23], a[252:255], v[116:119], v[20:23]\n" \
  "v_mfma_f32_16x16x32_bf16 v[16:19], a[252:255], v[112:115], v[16:19]\n" \
  "s_nop 0\n" \
  "s_nop 4\n" \
  "v_pk_fma_f32 v[0:1], v[40:41], v[48:49], v[0:1] op_sel:[0,0,0] op_sel_hi:[1,0,1]\n" \
  "v_pk_fma_f32 v[2:3], v[42:43], v[48:49], v[2:3] op_sel:[0,0,0] op_sel_hi:[1,0,1]\n" \
  "v_pk_fma_f32 v[16:17], v[44:45], v[48:49], v[16:17] op_sel:[0,0,0] op_sel_hi:[1,0,1]\n" \
  "v_pk_fma_f32 v[18:19], v[46:47], v[48:49], v[18:19] op_sel:[0,0,0] op_sel_hi:[1,0,1]\n" \
  "v_pk_mul_f32 v[0:1], v[0:1], v[32:33]\n" \
  "v_pk_mul_f32 v[2:3], v[2:3], v[34:35]\n" \
  "v_pk_mul_f32 v[16:17], v[16:17], v[36:37]\n" \
  "v_pk_mul_f32 v[18:19], v[18:19], v[38:39]\n" \
  "v_cvt_pk_bf16_f32 v56, v0, v1\n" \
  "v_cvt_pk_bf16_f32 v57, v2, v3\n" \
  "v_cvt_pk_bf16_f32 v58, v16, v17\n" \
  "v_cvt_pk_bf16_f32 v59, v18, v19\n" \
  "s_mov_b32 s74, 0\n" \
  "buffer_store_dwordx4 v[56:59], v151, s[40:43], s74 offen offset:0 sc1\n" \
  "v_pk_fma_f32 v[4:5], v[40:41], v[48:49], v[4:5] op_sel:[0,1,0] op_sel_hi:[1,1,1]\n" \
  "v_pk_fma_f32 v[6:7], v[42:43], v[48:49], v[6:7] op_sel:[0,1,0] op_sel_hi:[1,1,1]\n" \
  "v_pk_fma_f32 v[20:21], v[44:45], v[48:49], v[20:21] op_sel:[0,1,0] op_sel_hi:[1,1,1]\n" \
  "v_pk_fma_f32 v[22:23], v[46:47], v[48:49], v[22:23] op_sel:[0,1,0] op_sel_hi:[1,1,1]\n" \
  "v_pk_mul_f32 v[4:5], v[4:5], v[32:33]\n" \
  "v_pk_mul_f32 v[6:7], v[6:7], v[34:35]\n" \
  "v_pk_mul_f32 v[20:21], v[20:21], v[36:37]\n" \
  "v_pk_mul_f32 v[22:23], v[22:23], v[38:39]\n" \
  "v_cvt_pk_bf16_f32 v60, v4, v5\n" \
  "v_cvt_pk_bf16_f32 v61, v6, v7\n" \
  "v_cvt_pk_bf16_f32 v62, v20, v21\n" \
  "v_cvt_pk_bf16_f32 v63, v22, v23\n" \
  "buffer_store_dwordx4 v[60:63], v151, s[40:43], s74 offen offset:1024 sc1\n" \
  "v_pk_fma_f32 v[8:9], v[40:41], v[50:51], v[8:9] op_sel:[0,0,0] op_sel_hi:[1,0,1]\n" \
  "v_pk_fma_f32 v[10:11], v[42:43], v[50:51], v[10:11] op_sel:[0,0,0] op_sel_hi:[1,0,1]\n" \
  "v_pk_fma_f32 v[24:25], v[44:45], v[50:51], v[24:25] op_sel:[0,0,0] op_sel_hi:[1,0,1]\n" \
  "v_pk_fma_f32 v[26:27], v[46:47], v[50:51], v[26:27] op_sel:[0,0,0] op_sel_hi:[1,0,1]\n" \
  "v_pk_mul_f32 v[8:9], v[8:9], v[32:33]\n" \
  "v_pk_mul_f32 v[10:11], v[10:11], v[34:35]\n" \
  "v_pk_mul_f32 v[24:25], v[24:25], v[36:37]\n" \
  "v_pk_mul_f32 v[26:27], v[26:27], v[38:39]\n" \
  "v_cvt_pk_bf16_f32 v56, v8, v9\n" \
  "v_cvt_pk_bf16_f32 v57, v10, v11\n" \
  "v_cvt_pk_bf16_f32 v58, v24, v25\n" \
  "v_cvt_pk_bf16_f32 v59, v26, v27\n" \
  "s_mov_b32 s74, 65536\n" \
  "buffer_store_dwordx4 v[56:59], v151, s[40:43], s74 offen offset:0 sc1\n" \
  "v_pk_fma_f32 v[12:13], v[40:41], v[50:51], v[12:13] op_sel:[0,1,0] op_sel_hi:[1,1,1]\n" \
  "v_pk_fma_f32 v[14:15], v[42:43], v[50:51], v[14:15] op_sel:[0,1,0] op_sel_hi:[1,1,1]\n" \
  "v_pk_fma_f32 v[28:29], v[44:45], v[50:51], v[28:29] op_sel:[0,1,0] op_sel_hi:[1,1,1]\n" \
  "v_pk_fma_f32 v[30:31], v[46:47], v[50:51], v[30:31] op_sel:[0,1,0] op_sel_hi:[1,1,1]\n" \
  "v_pk_mul_f32 v[12:13], v[12:13], v[32:33]\n" \
  "v_pk_mul_f32 v[14:15], v[14:15], v[34:35]\n" \
  "v_pk_mul_f32 v[28:29], v[28:29], v[36:37]\n" \
  "v_pk_mul_f32 v[30:31], v[30:31], v[38:39]\n" \
  "v_cvt_pk_bf16_f32 v60, v12, v13\n" \
  "v_cvt_pk_bf16_f32 v61, v14, v15\n" \
  "v_cvt_pk_bf16_f32 v62, v28, v29\n" \
  "v_cvt_pk_bf16_f32 v63, v30, v31\n" \
  "buffer_store_dwordx4 v[60:63], v151, s[40:43], s74 offen offset:1024 sc1\n" \
  "s_waitcnt vmcnt(4)\n" \
  "s_barrier\n" \
  "ds_read_b128 v[64:67], v156 offset:32768\n" \
  "ds_read_b128 v[68:71], v156 offset:36864\n" \
  "ds_read_b128 v[72:75], v156 offset:40960\n" \
  "ds_read_b128 v[76:79], v156 offset:45056\n" \
  "ds_read_b128 v[80:83], v157 offset:32768\n" \
  "ds_read_b128 v[84:87], v157 offset:36864\n" \
  "ds_read_b128 v[88:91], v157 offset:40960\n" \
  "ds_read_b128 v[92:95], v157 offset:45056\n" \
  "s_waitcnt lgkmcnt(4)\n" \
  "v_mfma_f32_16x16x32_bf16 v[0:3], v[176:179], v[64:67], 0\n" \
  "ds_read_b128 v[96:99], v156 offset:34816\n" \
  "s_add_u32 m0, s73, 65536\n" \
  "v_mfma_f32_16x16x32_bf16 v[4:7], v[176:179], v[68:71], 0\n" \
  "s_add_u32 s72, s72, 128\n" \
  "v_mfma_f32_16x16x32_bf16 v[8:11], v[176:179], v[72:75], 0\n" \
  "ds_read_b128 v[100:103], v156 offset:38912\n" \
  "v_mfma_f32_16x16x32_bf16 v[12:15], v[176:179], v[76:79], 0\n" \
  "s_cmpk_eq_u32 s72, 1280\n" \
  "s_cselect_b32 s72, 0, s72\n" \
  "v_mfma_f32_16x16x32_bf16 v[28:31], v[180:183], v[76:79], 0\n" \
  "ds_read_b128 v[104:107], v156 offset:43008\n" \
  "buffer_load_dwordx4 v134, s[44:47], s72 offen offset:0 lds\n" \
  "v_mfma_f32_16x16x32_bf16 v[24:27], v[180:183], v[72:75], 0\n" \
  "v_mfma_f32_16x16x32_bf16 v[20:23], v[180:183], v[68:71], 0\n" \
  "ds_read_b128 v[108:111], v156 offset:47104\n" \
  "v_mfma_f32_16x16x32_bf16 v[16:19], v[180:183], v[64:67], 0\n" \
  "s_waitcnt lgkmcnt(4)\n" \
  "v_mfma_f32_16x16x32_bf16 v[0:3], v[184:187], v[80:83], v[0:3]\n" \
  "ds_read_b128 v[112:115], v157 offset:34816\n" \
  "buffer_load_dwordx4 v135, s[44:47], s72 offen offset:1024 lds\n" \
  "v_mfma_f32_16x16x32_bf16 v[4:7], v[184:187], v[84:87], v[4:7]\n" \
  "v_mfma_f32_16x16x32_bf16 v[8:11], v[184:187], v[88:91], v[8:11]\n" \
  "ds_read_b128 v[116:119], v157 offset:38912\n" \
  "v_mfma_f32_16x16x32_bf16 v[12:15], v[184:187], v[92:95], v[12:15]\n" \
  "v_mfma_f32_16x16x32_bf16 v[28:31], v[188:191], v[92:95], v[28:31]\n" \
  "ds_read_b128 v[120:123], v157 offset:43008\n" \
  "s_add_u32 m0, s68, 16384\n" \
  "v_mfma_f32_16x16x32_bf16 v[24:27], v[188:191], v[88:91], v[24:27]\n" \
  "s_add_u32 s72, s72, 128\n" \
  "v_mfma_f32_16x16x32_bf16 v[20:23], v[188:191], v[84:87], v[20:23]\n" \
  "ds_read_b128 v[124:127], v157 offset:47104\n" \
  "v_mfma_f32_16x16x32_bf16 v[16:19], v[188:191], v[80:83], v[16:19]\n" \
  "s_waitcnt lgkmcnt(4)\n" \
  "v_mfma_f32_16x16x32_bf16 v[0:3], v[192:195], v[96:99], v[0:3]\n" \
  "ds_read_b128 v[64:67], v146 offset:16384\n" \
  "v_mfma_f32_16x16x32_bf16 v[4:7], v[192:195], v[100:103], v[4:7]\n" \
  "s_cmpk_eq_u32 s72, 1280\n" \
  "s_cselect_b32 s72, 0, s72\n" \
  "v_mfma_f32_16x16x32_bf16 v[8:11], v[192:195], v[104:107], v[8:11]\n" \
  "ds_read_b128 v[68:71], v146 offset:18432\n" \
  "buffer_load_dwordx4 v134, s[44:47], s72 offen offset:0 lds\n" \
  "v_mfma_f32_16x16x32_bf16 v[12:15], v[192:195], v[108:111], v[12:15]\n" \
  "v_mfma_f32_16x16x32_bf16 v[28:31], v[196:199], v[108:111], v[28:31]\n" \
  "ds_read_b128 v[72:75], v146 offset:20480\n" \
  "v_mfma_f32_16x16x32_bf16 v[24:27], v[196:199], v[104:107], v[24:27]\n" \
  "v_mfma_f32_16x16x32_bf16 v[20:23], v[196:199], v[100:103], v[20:23]\n" \
  "ds_read_b128 v[76:79], v146 offset:22528\n" \
  "buffer_load_dwordx4 v135, s[44:47], s72 offen offset:1024 lds\n" \
  "v_mfma_f32_16x16x32_bf16 v[16:19], v[196:199], v[96:99], v[16:19]\n" \
  "s_waitcnt lgkmcnt(4)\n" \
  "v_mfma_f32_16x16x32_bf16 v[0:3], v[200:203], v[112:115], v[0:3]\n" \
  "ds_read_b128 v[80:83], v147 offset:16384\n" \
  "v_mfma_f32_16x16x32_bf16 v[4:7], v[200:203], v[116:119], v[4:7]\n" \
  "v_mfma_f32_16x16x32_bf16 v[8:11], v[200:203], v[120:123], v[8:11]\n" \
  "ds_read_b128 v[84:87], v147 offset:18432\n" \
  "s_add_u32 m0, s68, 98304\n" \
  "v_mfma_f32_16x16x32_bf16 v[12:15], v[200:203], v[124:127], v[12:15]\n" \
  "buffer_load_dwordx4 v136, s[48:51], s70 offen offset:0 lds\n" \
  "v_mfma_f32_16x16x32_bf16 v[28:31], v[204:207], v[124:127], v[28:31]\n" \
  "ds_read_b128 v[88:91], v147 offset:20480\n" \
  "v_mfma_f32_16x16x32_bf16 v[24:27], v[204:207], v[120:123], v[24:27]\n" \
  "v_mfma_f32_16x16x32_bf16 v[20:23], v[204:207], v[116:119], v[20:23]\n" \
  "ds_read_b128 v[92:95], v147 offset:22528\n" \
  "buffer_load_dwordx4 v137, s[48:51], s70 offen offset:1024 lds\n" \
  "v_mfma_f32_16x16x32_bf16 v[16:19], v[204:207], v[112:115], v[16:19]\n" \
  "s_waitcnt vmcnt(6) lgkmcnt(4)\n" \
  "s_barrier\n" \
  "v_mfma_f32_16x16x32_bf16 v[0:3], v[208:211], v[64:67], v[0:3]\n" \
  "ds_read_b128 v[96:99], v156 offset:49152\n" \
  "v_mfma_f32_16x16x32_bf16 v[4:7], v[208:211], v[68:71], v[4:7]\n" \
  "buffer_store_dword v166, v167, s[40:43], s86 offen sc1\n" \
  "v_mfma_f32_16x16x32_bf16 v[8:11], v[208:211], v[72:75], v[8:11]\n" \
  "ds_read_b128 v[100:103], v156 offset:53248\n" \
  "buffer_load_dwordx4 v138, s[48:51], s70 offen offset:2048 lds\n" \
  "v_mfma_f32_16x16x32_bf16 v[12:15], v[208:211], v[76:79], v[12:15]\n" \
  "v_mfma_f32_16x16x32_bf16 v[28:31], v[212:215], v[76:79], v[28:31]\n" \
  "ds_read_b128 v[104:107], v156 offset:57344\n" \
  "v_mfma_f32_16x16x32_bf16 v[24:27], v[212:215], v[72:75], v[24:27]\n" \
  "buffer_load_dwordx4 v139, s[48:51], s70 offen offset:3072 lds\n" \
  "v_mfma_f32_16x16x32_bf16 v[20:23], v[212:215], v[68:71], v[20:23]\n" \
  "ds_read_b128 v[108:111], v156 offset:61440\n" \
  "s_add_u32 s70, s70, 128\n" \
  "v_mfma_f32_16x16x32_bf16 v[16:19], v[212:215], v[64:67], v[16:19]\n" \
  "s_and_b32 s70, s70, 2047\n" \
  "s_waitcnt lgkmcnt(4)\n" \
  "v_mfma_f32_16x16x32_bf16 v[0:3], v[216:219], v[80:83], v[0:3]\n" \
  "ds_read_b128 v[112:115], v157 offset:49152\n" \
  "s_add_u32 m0, s68, 114688\n" \
  "v_mfma_f32_16x16x32_bf16 v[4:7], v[216:219], v[84:87], v[4:7]\n" \
  "buffer_load_dwordx4 v136, s[48:51], s70 offen offset:0 lds\n" \
  "v_mfma_f32_16x16x32_bf16 v[8:11], v[216:219], v[88:91], v[8:11]\n" \
  "ds_read_b128 v[116:119], v157 offset:53248\n" \
  "v_mfma_f32_16x16x32_bf16 v[12:15], v[216:219], v[92:95], v[12:15]\n" \
  "v_mfma_f32_16x16x32_bf16 v[28:31], v[220:223], v[92:95], v[28:31]\n" \
  "ds_read_b128 v[120:123], v157 offset:57344\n" \
  "buffer_load_dwordx4 v137, s[48:51], s70 offen offset:1024 lds\n" \
  "v_mfma_f32_16x16x32_bf16 v[24:27], v[220:223], v[88:91], v[24:27]\n" \
  "v_mfma_f32_16x16x32_bf16 v[20:23], v[220:223], v[84:87], v[20:23]\n" \
  "ds_read_b128 v[124:127], v157 offset:61440\n" \
  "v_mfma_f32_16x16x32_bf16 v[16:19], v[220:223], v[80:83], v[16:19]\n" \
  "buffer_load_dwordx4 v138, s[48:51], s70 offen offset:2048 lds\n" \
  "s_waitcnt lgkmcnt(4)\n" \
  "v_mfma_f32_16x16x32_bf16 v[0:3], v[224:227], v[96:99], v[0:3]\n" \
  "ds_read_b128 v[64:67], v156 offset:51200\n" \
  "v_mfma_f32_16x16x32_bf16 v[4:7], v[224:227], v[100:103], v[4:7]\n" \
  "v_mfma_f32_16x16x32_bf16 v[8:11], v[224:227], v[104:107], v[8:11]\n" \
  "ds_read_b128 v[68:71], v156 offset:55296\n" \
  "buffer_load_dwordx4 v139, s[48:51], s70 offen offset:3072 lds\n" \
  "v_mfma_f32_16x16x32_bf16 v[12:15], v[224:227], v[108:111], v[12:15]\n" \
  "s_add_u32 s70, s70, 128\n" \
  "s_and_b32 s70, s70, 2047\n" \
  "v_mfma_f32_16x16x32_bf16 v[28:31], v[228:231], v[108:111], v[28:31]\n" \
  "ds_read_b128 v[72:75], v156 offset:59392\n" \
  "v_mfma_f32_16x16x32_bf16 v[24:27], v[228:231], v[104:107], v[24:27]\n" \
  "s_add_u32 m0, s68, 131072\n" \
  "v_mfma_f32_16x16x32_bf16 v[20:23], v[228:231], v[100:103], v[20:23]\n" \
  "ds_read_b128 v[76:79], v156 offset:63488\n" \
  "buffer_load_dwordx4 v136, s[48:51], s70 offen offset:0 lds\n" \
  "v_mfma_f32_16x16x32_bf16 v[16:19], v[228:231], v[96:99], v[16:19]\n" \
  "s_waitcnt lgkmcnt(4)\n" \
  "v_mfma_f32_16x16x32_bf16 v[0:3], v[232:235], v[112:115], v[0:3]\n" \
  "ds_read_b128 v[80:83], v157 offset:51200\n" \
  "v_mfma_f32_16x16x32_bf16 v[4:7], v[232:235], v[116:119], v[4:7]\n" \
  "buffer_load_dwordx4 v137, s[48:51], s70 offen offset:1024 lds\n" \
  "v_mfma_f32_16x16x32_bf16 v[8:11], v[232:235], v[120:123], v[8:11]\n" \
  "ds_read_b128 v[84:87], v157 offset:55296\n" \
  "v_mfma_f32_16x16x32_bf16 v[12:15], v[232:235], v[124:127], v[12:15]\n" \
  "v_mfma_f32_16x16x32_bf16 v[28:31], v[236:239], v[124:127], v[28:31]\n" \
  "ds_read_b128 v[88:91], v157 offset:59392\n" \
  "buffer_load_dwordx4 v138, s[48:51], s70 offen offset:2048 lds\n" \
  "v_mfma_f32_16x16x32_bf16 v[24:27], v[236:239], v[120:123], v[24:27]\n" \
  "v_mfma_f32_16x16x32_bf16 v[20:23], v[236:239], v[116:119], v[20:23]\n" \
  "ds_read_b128 v[92:95], v157 offset:63488\n" \
  "v_mfma_f32_16x16x32_bf16 v[16:19], v[236:239], v[112:115], v[16:19]\n" \
  "buffer_load_dwordx4 v139, s[48:51], s70 offen offset:3072 lds\n" \
  "s_waitcnt lgkmcnt(4)\n" \
  "v_mfma_f32_16x16x32_bf16 v[0:3], a[160:163], v[64:67], v[0:3]\n" \
  "ds_read_b128 v[96:99], v146 offset:24576\n" \
  "s_add_u32 s70, s70, 128\n" \
  "v_mfma_f32_16x16x32_bf16 v[4:7], a[160:163], v[68:71], v[4:7]\n" \
  "s_and_b32 s70, s70, 2047\n" \
  "v_mfma_f32_16x16x32_bf16 v[8:11], a[160:163], v[72:75], v[8:11]\n" \
  "ds_read_b128 v[100:103], v146 offset:26624\n" \
  "s_add_u32 m0, s68, 147456\n" \
  "v_mfma_f32_16x16x32_bf16 v[12:15], a[160:163], v[76:79], v[12:15]\n" \
  "buffer_load_dwordx4 v136, s[48:51], s70 offen offset:0 lds\n" \
  "v_mfma_f32_16x16x32_bf16 v[28:31], a[164:167], v[76:79], v[28:31]\n" \
  "ds_read_b128 v[104:107], v146 offset:28672\n" \
  "v_mfma_f32_16x16x32_bf16 v[24:27], a[164:167], v[72:75], v[24:27]\n" \
  "v_mfma_f32_16x16x32_bf16 v[20:23], a[164:167], v[68:71], v[20:23]\n" \
  "ds_read_b128 v[108:111], v146 offset:30720\n" \
  "buffer_load_dwordx4 v137, s[48:51], s70 offen offset:1024 lds\n" \
  "v_mfma_f32_16x16x32_bf16 v[16:19], a[164:167], v[64:67], v[16:19]\n" \
  "PAD1_%=:\n" \
  "s_cbranch_execz PAD5_%=\n" \
  "s_waitcnt lgkmcnt(4)\n" \
  "v_mfma_f32_16x16x32_bf16 v[0:3], a[168:171], v[80:83], v[0:3]\n" \
  "ds_read_b128 v[112:115], v147 offset:24576\n" \
  "v_mfma_f32_16x16x32_bf16 v[4:7], a[168:171], v[84:87], v[4:7]\n" \
  "buffer_load_dwordx4 v138, s[48:51], s70 offen offset:2048 lds\n" \
  "v_mfma_f32_16x16x32_bf16 v[8:11], a[168:171], v[88:91], v[8:11]\n" \
  "ds_read_b128 v[116:119], v147 offset:26624\n" \
  "v_mfma_f32_16x16x32_bf16 v[12:15], a[168:171], v[92:95], v[12:15]\n" \
  "v_mfma_f32_16x16x32_bf16 v[28:31], a[172:175], v[92:95], v[28:31]\n" \
  "ds_read_b128 v[120:123], v147 offset:28672\n" \
  "buffer_load_dwordx4 v139, s[48:51], s70 offen offset:3072 lds\n" \
  "v_mfma_f32_16x16x32_bf16 v[24:27], a[172:175], v[88:91], v[24:27]\n" \
  "s_add_u32 s70, s70, 128\n" \
  "s_and_b32 s70, s70, 2047\n" \
  "v_mfma_f32_16x16x32_bf16 v[20:23], a[172:175], v[84:87], v[20:23]\n" \
  "ds_read_b128 v[124:127], v147 offset:30720\n" \
  "s_waitcnt vmcnt(12)\n" \
  "v_mfma_f32_16x16x32_bf16 v[16:19], a[172:175], v[80:83], v[16:19]\n" \
  "ds_read_b128 a[0:3], v140 offset:0\n" \
  "s_waitcnt lgkmcnt(5)\n" \
  "v_mfma_f32_16x16x32_bf16 v[0:3], a[176:179], v[96:99], v[0:3]\n" \
  "ds_read_b128 v[64:67], v156 offset:0\n" \
  "v_mfma_f32_16x16x32_bf16 v[4:7], a[176:179], v[100:103], v[4:7]\n" \
  "ds_read_b128 a[4:7], v141 offset:0\n" \
  "v_mfma_f32_16x16x32_bf16 v[8:11], a[176:179], v[104:107], v[8:11]\n" \
  "ds_read_b128 v[68:71], v156 offset:4096\n" \
  "v_mfma_f32_16x16x32_bf16 v[12:15], a[176:179], v[108:111], v[12:15]\n" \
  "ds_read_b128 a[8:11], v142 offset:0\n" \
  "v_mfma_f32_16x16x32_bf16 v[28:31], a[180:183], v[108:111], v[28:31]\n" \
  "ds_read_b128 v[72:75], v156 offset:8192\n" \
  "v_mfma_f32_16x16x32_bf16 v[24:27], a[180:183], v[104:107], v[24:27]\n" \
  "ds_read_b128 a[12:15], v143 offset:0\n" \
  "v_mfma_f32_16x16x32_bf16 v[20:23], a[180:183], v[100:103], v[20:23]\n" \
  "ds_read_b128 v[76:79], v156 offset:12288\n" \
  "v_mfma_f32_16x16x32_bf16 v[16:19], a[180:183], v[96:99], v[16:19]\n" \
  "s_waitcnt lgkmcnt(8)\n" \
  "v_mfma_f32_16x16x32_bf16 v[0:3], a[184:187], v[112:115], v[0:3]\n" \
  "ds_read_b128 v[80:83], v157 offset:0\n" \
  "s_waitcnt lgkmcnt(2)\n" \
  "v_mfma_f32_16x16x32_bf16 v[4:7], a[184:187], v[116:119], v[4:7]\n" \
  "s_add_u32 m0, s68, 98304\n" \
  "v_mfma_f32_16x16x32_bf16 v[8:11], a[184:187], v[120:123], v[8:11]\n" \
  "ds_read_b128 v[84:87], v157 offset:4096\n" \
  "buffer_load_dwordx4 v136, s[48:51], s70 offen offset:0 lds\n" \
  "v_mfma_f32_16x16x32_bf16 v[12:15], a[184:187], v[124:127], v[12:15]\n" \
  "v_mfma_f32_16x16x32_bf16 v[28:31], a[188:191], v[124:127], v[28:31]\n" \
  "ds_read_b128 v[88:91], v157 offset:8192\n" \
  "s_waitcnt vmcnt(9)\n" \
  "v_mfma_f32_16x16x32_bf16 v[24:27], a[188:191], v[120:123], v[24:27]\n" \
  "buffer_load_dwordx4 v137, s[48:51], s70 offen offset:1024 lds\n" \
  "ds_read_b128 a[16:19], v140 offset:16384\n" \
  "v_mfma_f32_16x16x32_bf16 v[20:23], a[188:191], v[116:119], v[20:23]\n" \
  "ds_read_b128 v[92:95], v157 offset:12288\n" \
  "v_mfma_f32_16x16x32_bf16 v[16:19], a[188:191], v[112:115], v[16:19]\n" \
  "ds_read_b128 a[20:23], v141 offset:16384\n" \
  "s_waitcnt lgkmcnt(6)\n" \
  "v_mfma_f32_16x16x32_bf16 v[0:3], a[192:195], v[64:67], v[0:3]\n" \
  "ds_read_b128 v[96:99], v156 offset:2048\n" \
  "buffer_load_dwordx4 v138, s[48:51], s70 offen offset:2048 lds\n" \
  "v_mfma_f32_16x16x32_bf16 v[4:7], a[192:195], v[68:71], v[4:7]\n" \
  "ds_read_b128 a[24:27], v142 offset:16384\n" \
  "v_mfma_f32_16x16x32_bf16 v[8:11], a[192:195], v[72:75], v[8:11]\n" \
  "ds_read_b128 v[100:103], v156 offset:6144\n" \
  "v_mfma_f32_16x16x32_bf16 v[12:15], a[192:195], v[76:79], v[12:15]\n" \
  "buffer_load_dwordx4 v139, s[48:51], s70 offen offset:3072 lds\n" \
  "ds_read_b128 a[28:31], v143 offset:16384\n" \
  "v_mfma_f32_16x16x32_bf16 v[28:31], a[196:199], v[76:79], v[28:31]\n" \
  "ds_read_b128 v[104:107], v156 offset:10240\n" \
  "s_add_u32 s70, s70, 128\n" \
  "v_mfma_f32_16x16x32_bf16 v[24:27], a[196:199], v[72:75], v[24:27]\n" \
  "s_and_b32 s70, s70, 2047\n" \
  "v_mfma_f32_16x16x32_bf16 v[20:23], a[196:199], v[68:71], v[20:23]\n" \
  "ds_read_b128 v[108:111], v156 offset:14336\n" \
  "s_waitcnt lgkmcnt(2)\n" \
  "v_mfma_f32_16x16x32_bf16 v[16:19], a[196:199], v[64:67], v[16:19]\n" \
  "s_add_u32 m0, s68, 114688\n" \
  "s_nop 0\n" \
  "v_mfma_f32_16x16x32_bf16 v[0:3], a[200:203], v[80:83], v[0:3]\n" \
  "ds_read_b128 v[112:115], v157 offset:2048\n" \
  "buffer_load_dwordx4 v136, s[48:51], s70 offen offset:0 lds\n" \
  "v_mfma_f32_16x16x32_bf16 v[4:7], a[200:203], v[84:87], v[4:7]\n" \
  "v_mfma_f32_16x16x32_bf16 v[8:11], a[200:203], v[88:91], v[8:11]\n" \
  "ds_read_b128 v[116:119], v157 offset:6144\n" \
  "s_waitcnt vmcnt(9)\n" \
  "v_mfma_f32_16x16x32_bf16 v[12:15], a[200:203], v[92:95], v[12:15]\n" \
  "buffer_load_dwordx4 v137, s[48:51], s70 offen offset:1024 lds\n" \
  "ds_read_b128 a[32:35], v140 offset:32768\n" \
  "v_mfma_f32_16x16x32_bf16 v[28:31], a[204:207], v[92:95], v[28:31]\n" \
  "ds_read_b128 v[120:123], v157 offset:10240\n" \
  "v_mfma_f32_16x16x32_bf16 v[24:27], a[204:207], v[88:91], v[24:27]\n" \
  "ds_read_b128 a[36:39], v141 offset:32768\n" \
  "v_mfma_f32_16x16x32_bf16 v[20:23], a[204:207], v[84:87], v[20:23]\n" \
  "ds_read_b128 v[124:127], v157 offset:14336\n" \
  "buffer_load_dwordx4 v138, s[48:51], s70 offen offset:2048 lds\n" \
  "v_mfma_f32_16x16x32_bf16 v[16:19], a[204:207], v[80:83], v[16:19]\n" \
  "s_waitcnt lgkmcnt(6)\n" \
  "s_barrier\n" \
  "v_mfma_f32_16x16x32_bf16 v[0:3], a[208:211], v[96:99], v[0:3]\n" \
  "ds_read_b128 v[64:67], v146 offset:0\n" \
  "v_mfma_f32_16x16x32_bf16 v[4:7], a[208:211], v[100:103], v[4:7]\n" \
  "buffer_load_dwordx4 v139, s[48:51], s70 offen offset:3072 lds\n" \
  "ds_read_b128 a[40:43], v142 offset:32768\n" \
  "v_mfma_f32_16x16x32_bf16 v[8:11], a[208:211], v[104:107], v[8:11]\n" \
  "ds_read_b128 v[68:71], v146 offset:2048\n" \
  "s_add_u32 s70, s70, 128\n" \
  "v_mfma_f32_16x16x32_bf16 v[12:15], a[208:211], v[108:111], v[12:15]\n" \
  "s_and_b32 s70, s70, 2047\n" \
  "ds_read_b128 a[44:47], v143 offset:32768\n" \
  "v_mfma_f32_16x16x32_bf16 v[28:31], a[212:215], v[108:111], v[28:31]\n" \
  "ds_read_b128 v[72:75], v146 offset:4096\n" \
  "v_mfma_f32_16x16x32_bf16 v[24:27], a[212:215], v[104:107], v[24:27]\n" \
  "v_mfma_f32_16x16x32_bf16 v[20:23], a[212:215], v[100:103], v[20:23]\n" \
  "ds_read_b128 v[76:79], v146 offset:6144\n" \
  "s_waitcnt lgkmcnt(2)\n" \
  "v_mfma_f32_16x16x32_bf16 v[16:19], a[212:215], v[96:99], v[16:19]\n" \
  "s_add_u32 m0, s68, 131072\n" \
  "s_nop 0\n" \
  "v_mfma_f32_16x16x32_bf16 v[0:3], a[216:219], v[112:115], v[0:3]\n" \
  "ds_read_b128 v[80:83], v147 offset:0\n" \
  "buffer_load_dwordx4 v136, s[48:51], s70 offen offset:0 lds\n" \
  "v_mfma_f32_16x16x32_bf16 v[4:7], a[216:219], v[116:119], v[4:7]\n" \
  "v_mfma_f32_16x16x32_bf16 v[8:11], a[216:219], v[120:123], v[8:11]\n" \
  "ds_read_b128 v[84:87], v147 offset:2048\n" \
  "s_waitcnt vmcnt(9)\n" \
  "v_mfma_f32_16x16x32_bf16 v[12:15], a[216:219], v[124:127], v[12:15]\n" \
  "buffer_load_dwordx4 v137, s[48:51], s70 offen offset:1024 lds\n" \
  "ds_read_b128 a[48:51], v140 offset:49152\n" \
  "v_mfma_f32_16x16x32_bf16 v[28:31], a[220:223], v[124:127], v[28:31]\n" \
  "ds_read_b128 v[88:91], v147 offset:4096\n" \
  "v_mfma_f32_16x16x32_bf16 v[24:27], a[220:223], v[120:123], v[24:27]\n" \
  "ds_read_b128 a[52:55], v141 offset:49152\n" \
  "v_mfma_f32_16x16x32_bf16 v[20:23], a[220:223], v[116:119], v[20:23]\n" \
  "ds_read_b128 v[92:95], v147 offset:6144\n" \
  "buffer_load_dwordx4 v138, s[48:51], s70 offen offset:2048 lds\n" \
  "v_mfma_f32_16x16x32_bf16 v[16:19], a[220:223], v[112:115], v[16:19]\n" \
  "ds_read_b128 a[56:59], v142 offset:49152\n" \
  "s_waitcnt lgkmcnt(7)\n" \
  "v_mfma_f32_16x16x32_bf16 v[0:3], a[224:227], v[64:67], v[0:3]\n" \
  "ds_read_b128 v[96:99], v156 offset:16384\n" \
  "v_mfma_f32_16x16x32_bf16 v[4:7], a[224:227], v[68:71], v[4:7]\n" \
  "buffer_load_dwordx4 v139, s[48:51], s70 offen offset:3072 lds\n" \
  "ds_read_b128 a[60:63], v143 offset:49152\n" \
  "v_mfma_f32_16x16x32_bf16 v[8:11], a[224:227], v[72:75], v[8:11]\n" \
  "ds_read_b128 v[100:103], v156 offset:20480\n" \
  "s_add_u32 s70, s70, 128\n" \
  "v_mfma_f32_16x16x32_bf16 v[12:15], a[224:227], v[76:79], v[12:15]\n" \
  "s_and_b32 s70, s70, 2047\n" \
  "v_mfma_f32_16x16x32_bf16 v[28:31], a[228:231], v[76:79], v[28:31]\n" \
  "ds_read_b128 v[104:107], v156 offset:24576\n" \
  "s_waitcnt lgkmcnt(2)\n" \
  "v_mfma_f32_16x16x32_bf16 v[24:27], a[228:231], v[72:75], v[24:27]\n" \
  "s_add_u32 m0, s68, 147456\n" \
  "v_mfma_f32_16x16x32_bf16 v[20:23], a[228:231], v[68:71], v[20:23]\n" \
  "ds_read_b128 v[108:111], v156 offset:28672\n" \
  "buffer_load_dwordx4 v136, s[48:51], s70 offen offset:0 lds\n" \
  "v_mfma_f32_16x16x32_bf16 v[16:19], a[228:231], v[64:67], v[16:19]\n" \
  "s_nop 0\n" \
  "v_mfma_f32_16x16x32_bf16 v[0:3], a[232:235], v[80:83], v[0:3]\n" \
  "ds_read_b128 v[112:115], v157 offset:16384\n" \
  "v_mfma_f32_16x16x32_bf16 v[4:7], a[232:235], v[84:87], v[4:7]\n" \
  "buffer_load_dwordx4 v137, s[48:51], s70 offen offset:1024 lds\n" \
  "v_mfma_f32_16x16x32_bf16 v[8:11], a[232:235], v[88:91], v[8:11]\n" \
  "ds_read_b128 v[116:119], v157 offset:20480\n" \
  "v_mfma_f32_16x16x32_bf16 v[12:15], a[232:235], v[92:95], v[12:15]\n" \
  "v_mfma_f32_16x16x32_bf16 v[28:31], a[236:239], v[92:95], v[28:31]\n" \
  "ds_read_b128 v[120:123], v157 offset:24576\n" \
  "buffer_load_dwordx4 v138, s[48:51], s70 offen offset:2048 lds\n" \
  "v_mfma_f32_16x16x32_bf16 v[24:27], a[236:239], v[88:91], v[24:27]\n" \
  "v_mfma_f32_16x16x32_bf16 v[20:23], a[236:239], v[84:87], v[20:23]\n" \
  "ds_read_b128 v[124:127], v157 offset:28672\n" \
  "v_mfma_f32_16x16x32_bf16 v[16:19], a[236:239], v[80:83], v[16:19]\n" \
  "buffer_load_dwordx4 v139, s[48:51], s70 offen offset:3072 lds\n" \
  "s_waitcnt lgkmcnt(4)\n" \
  "v_mfma_f32_16x16x32_bf16 v[0:3], a[240:243], v[96:99], v[0:3]\n" \
  "s_add_u32 s70, s70, 128\n" \
  "s_and_b32 s70, s70, 2047\n" \
  "v_mfma_f32_16x16x32_bf16 v[4:7], a[240:243], v[100:103], v[4:7]\n" \
  "v_mfma_f32_16x16x32_bf16 v[8:11], a[240:243], v[104:107], v[8:11]\n" \
  "v_mfma_f32_16x16x32_bf16 v[12:15], a[240:243], v[108:111], v[12:15]\n" \
  "v_mfma_f32_16x16x32_bf16 v[28:31], a[244:247], v[108:111], v[28:31]\n" \
  "v_mfma_f32_16x16x32_bf16 v[24:27], a[244:247], v[104:107], v[24:27]\n" \
  "v_mfma_f32_16x16x32_bf16 v[20:23], a[244:247], v[100:103], v[20:23]\n" \
  "v_mfma_f32_16x16x32_bf16 v[16:19], a[244:247], v[96:99], v[16:19]\n" \
  "s_waitcnt lgkmcnt(0)\n" \
  "v_mfma_f32_16x16x32_bf16 v[0:3], a[248:251], v[112:115], v[0:3]\n" \
  "v_mfma_f32_16x16x32_bf16 v[4:7], a[248:251], v[116:119], v[4:7]\n" \
  "v_mfma_f32_16x16x32_bf16 v[8:11], a[248:251], v[120:123], v[8:11]\n" \
  "v_mfma_f32_16x16x32_bf16 v[12:15], a[248:251], v[124:127], v[12:15]\n" \
  "v_mfma_f32_16x16x32_bf16 v[28:31], a[252:255], v[124:127], v[28:31]\n" \
  "v_mfma_f32_16x16x32_bf16 v[24:27], a[252:255], v[120:123], v[24:27]\n" \
  "v_mfma_f32_16x16x32_bf16 v[20:23], a[252:255], v[116:119], v[20:23]\n" \
  "v_mfma_f32_16x16x32_bf16 v[16:19], a[252:255], v[112:115], v[16:19]\n" \
  "s_nop 7\n" \
  "v_pk_fma_f32 v[0:1], v[40:41], v[52:53], v[0:1] op_sel:[0,0,0] op_sel_hi:[1,0,1]\n" \
  "v_pk_fma_f32 v[2:3], v[42:43], v[52:53], v[2:3] op_sel:[0,0,0] op_sel_hi:[1,0,1]\n" \
  "v_pk_fma_f32 v[16:17], v[44:45], v[52:53], v[16:17] op_sel:[0,0,0] op_sel_hi:[1,0,1]\n" \
  "v_pk_fma_f32 v[18:19], v[46:47], v[52:53], v[18:19] op_sel:[0,0,0] op_sel_hi:[1,0,1]\n" \
  "v_pk_mul_f32 v[0:1], v[0:1], v[32:33]\n" \
  "v_pk_mul_f32 v[2:3], v[2:3], v[34:35]\n" \
  "v_pk_mul_f32 v[16:17], v[16:17], v[36:37]\n" \
  "v_pk_mul_f32 v[18:19], v[18:19], v[38:39]\n" \
  "v_cvt_pk_bf16_f32 v56, v0, v1\n" \
  "v_cvt_pk_bf16_f32 v57, v2, v3\n" \
  "v_cvt_pk_bf16_f32 v58, v16, v17\n" \
  "v_cvt_pk_bf16_f32 v59, v18, v19\n" \
  "s_mov_b32 s74, 655360\n" \
  "buffer_store_dwordx4 v[56:59], v151, s[40:43], s74 offen offset:0 sc1\n" \
  "v_pk_fma_f32 v[4:5], v[40:41], v[52:53], v[4:5] op_sel:[0,1,0] op_sel_hi:[1,1,1]\n" \
  "v_pk_fma_f32 v[6:7], v[42:43], v[52:53], v[6:7] op_sel:[0,1,0] op_sel_hi:[1,1,1]\n" \
  "v_pk_fma_f32 v[20:21], v[44:45], v[52:53], v[20:21] op_sel:[0,1,0] op_sel_hi:[1,1,1]\n" \
  "v_pk_fma_f32 v[22:23], v[46:47], v[52:53], v[22:23] op_sel:[0,1,0] op_sel_hi:[1,1,1]\n" \
  "v_pk_mul_f32 v[4:5], v[4:5], v[32:33]\n" \
  "v_pk_mul_f32 v[6:7], v[6:7], v[34:35]\n" \
  "v_pk_mul_f32 v[20:21], v[20:21], v[36:37]\n" \
  "v_pk_mul_f32 v[22:23], v[22:23], v[38:39]\n" \
  "v_cvt_pk_bf16_f32 v60, v4, v5\n" \
  "v_cvt_pk_bf16_f32 v61, v6, v7\n" \
  "v_cvt_pk_bf16_f32 v62, v20, v21\n" \
  "v_cvt_pk_bf16_f32 v63, v22, v23\n" \
  "buffer_store_dwordx4 v[60:63], v151, s[40:43], s74 offen offset:1024 sc1\n" \
  "v_pk_fma_f32 v[8:9], v[40:41], v[54:55], v[8:9] op_sel:[0,0,0] op_sel_hi:[1,0,1]\n" \
  "v_pk_fma_f32 v[10:11], v[42:43], v[54:55], v[10:11] op_sel:[0,0,0] op_sel_hi:[1,0,1]\n" \
  "v_pk_fma_f32 v[24:25], v[44:45], v[54:55], v[24:25] op_sel:[0,0,0] op_sel_hi:[1,0,1]\n" \
  "v_pk_fma_f32 v[26:27], v[46:47], v[54:55], v[26:27] op_sel:[0,0,0] op_sel_hi:[1,0,1]\n" \
  "v_pk_mul_f32 v[8:9], v[8:9], v[32:33]\n" \
  "v_pk_mul_f32 v[10:11], v[10:11], v[34:35]\n" \
  "v_pk_mul_f32 v[24:25], v[24:25], v[36:37]\n" \
  "v_pk_mul_f32 v[26:27], v[26:27], v[38:39]\n" \
  "v_cvt_pk_bf16_f32 v56, v8, v9\n" \
  "v_cvt_pk_bf16_f32 v57, v10, v11\n" \
  "v_cvt_pk_bf16_f32 v58, v24, v25\n" \
  "v_cvt_pk_bf16_f32 v59, v26, v27\n" \
  "s_mov_b32 s74, 720896\n" \
  "buffer_store_dwordx4 v[56:59], v151, s[40:43], s74 offen offset:0 sc1\n" \
  "v_pk_fma_f32 v[12:13], v[40:41], v[54:55], v[12:13] op_sel:[0,1,0] op_sel_hi:[1,1,1]\n" \
  "v_pk_fma_f32 v[14:15], v[42:43], v[54:55], v[14:15] op_sel:[0,1,0] op_sel_hi:[1,1,1]\n" \
  "v_pk_fma_f32 v[28:29], v[44:45], v[54:55], v[28:29] op_sel:[0,1,0] op_sel_hi:[1,1,1]\n" \
  "v_pk_fma_f32 v[30:31], v[46:47], v[54:55], v[30:31] op_sel:[0,1,0] op_sel_hi:[1,1,1]\n" \
  "v_pk_mul_f32 v[12:13], v[12:13], v[32:33]\n" \
  "v_pk_mul_f32 v[14:15], v[14:15], v[34:35]\n" \
  "v_pk_mul_f32 v[28:29], v[28:29], v[36:37]\n" \
  "v_pk_mul_f32 v[30:31], v[30:31], v[38:39]\n" \
  "v_cvt_pk_bf16_f32 v60, v12, v13\n" \
  "v_cvt_pk_bf16_f32 v61, v14, v15\n" \
  "v_cvt_pk_bf16_f32 v62, v28, v29\n" \
  "v_cvt_pk_bf16_f32 v63, v30, v31\n" \
  "buffer_store_dwordx4 v[60:63], v151, s[40:43], s74 offen offset:1024 sc1\n" \
  "v_add_u32 v247, 655360, v151\n" \
  "s_waitcnt vmcnt(16) lgkmcnt(0)\n" \
  "ds_read_b128 a[64:67], v140 offset:0\n" \
  "ds_read_b128 a[68:71], v141 offset:0\n" \
  "ds_read_b128 a[72:75], v142 offset:0\n" \
  "ds_read_b128 a[76:79], v143 offset:0\n" \
  "PBEG_%=:\n" \
  "PAD2_%=:\n" \
  "s_cbranch_execz PAD6_%=\n" \
  "s_bfe_u32 s76, %[bid], 0x20001\n" \
  "s_bfe_u32 s77, %[bid], 0x20003\n" \
  "s_lshr_b32 s79, s68, 12\n" \
  "v_and_b32 v52, 63, %[tid]\n" \
  "v_cmp_gt_u32 vcc, 4, v52\n" \
  "v_mov_b32 v49, 262144\n" \
  "s_mul_i32 s60, s76, 20\n" \
  "s_and_b32 s61, s77, 2\n" \
  "s_add_u32 s60, s60, s61\n" \
  "s_and_b32 s61, s79, 2\n" \
  "s_lshl_b32 s61, s61, 1\n" \
  "s_add_u32 s60, s60, s61\n" \
  "s_and_b32 s61, s79, 1\n" \
  "s_add_u32 s60, s60, s61\n" \
  "s_lshl_b32 s60, s60, 2\n" \
  "s_add_u32 s60, s60, 384\n" \
  "v_cndmask_b32 v52, v49, v52, vcc\n" \
  "v_add_lshl_u32 v134, v52, s60, 7\n" \
  "buffer_load_dword v52, v134, s[40:43], 0 offen sc1\n" \
  "v_and_b32 v53, 31, %[tid]\n" \
  "v_bfe_u32 v54, %[tid], 3, 3\n" \
  "v_sub_u32 v53, v53, v54\n" \
  "v_lshl_add_u32 v51, v53, 12, v136\n" \
  "v_and_b32 v54, 32, %[tid]\n" \
  "v_lshl_add_u32 v51, v54, 2, v51\n" \
  "s_lshr_b32 s87, s77, 1\n" \
  "v_lshl_add_u32 v51, s87, 24, v51\n" \
  "s_lshl_b32 s87, s77, 8\n" \
  "s_add_u32 s87, s87, s70\n" \
  "s_add_u32 s87, s87, 512\n" \
  "s_and_b32 s87, s87, 2047\n" \
  "s_sub_u32 s83, s70, 1024\n" \
  "s_and_b32 s83, s83, 2047\n" \
  "s_lshl_b32 s83, s83, 5\n" \
  "s_lshl_b32 s61, s61, 12\n" \
  "s_add_u32 s80, s83, s61\n" \
  "s_and_b32 s80, s80, 0xffff\n" \
  "s_mul_i32 s60, s76, 20\n" \
  "s_add_u32 s60, s60, s77\n" \
  "s_lshl_b32 s60, s60, 16\n" \
  "s_add_u32 s60, s60, 1048576\n" \
  "s_add_u32 s75, s60, 0x80000\n" \
  "s_lshr_b32 s61, s79, 1\n" \
  "s_lshl_b32 s61, s61, 18\n" \
  "s_add_u32 s81, s60, s61\n" \
  "v_and_b32 v128, 63, %[tid]\n" \
  "v_lshlrev_b32 v128, 4, v128\n" \
  "v_add_u32 v129, 0x10000, v128\n" \
  "v_mov_b32 v135, s71\n" \
  "v_cndmask_b32 v49, 0, v135, vcc\n" \
  "v_and_b32 v50, 32, %[tid]\n" \
  "v_lshlrev_b32 v50, 22, v50\n" \
  "v_or_b32 v50, v50, v128\n" \
  "v_lshrrev_b32 v50, 2, v50\n" \
  "s_waitcnt vmcnt(9) lgkmcnt(0)\n" \
  "ds_read_b128 a[80:83], v140 offset:16384\n" \
  "ds_read_b128 a[84:87], v141 offset:16384\n" \
  "ds_read_b128 a[88:91], v142 offset:16384\n" \
  "ds_read_b128 a[92:95], v143 offset:16384\n" \
  "s_add_u32 m0, s68, 98304\n" \
  "s_nop 0\n" \
  "buffer_load_dwordx4 v136, s[48:51], s70 offen offset:0 lds\n" \
  "buffer_load_dwordx4 v137, s[48:51], s70 offen offset:1024 lds\n" \
  "buffer_load_dwordx4 v138, s[48:51], s70 offen offset:2048 lds\n" \
  "buffer_load_dwordx4 v139, s[48:51], s70 offen offset:3072 lds\n" \
  "s_add_u32 s70, s70, 128\n" \
  "s_and_b32 s70, s70, 2047\n" \
  "s_waitcnt vmcnt(9) lgkmcnt(0)\n" \
  "ds_read_b128 a[96:99], v140 offset:32768\n" \
  "ds_read_b128 a[100:103], v141 offset:32768\n" \
  "ds_read_b128 a[104:107], v142 offset:32768\n" \
  "ds_read_b128 a[108:111], v143 offset:32768\n" \
  "s_add_u32 m0, s68, 114688\n" \
  "s_nop 0\n" \
  "buffer_load_dwordx4 v136, s[48:51], s70 offen offset:0 lds\n" \
  "buffer_load_dwordx4 v137, s[48:51], s70 offen offset:1024 lds\n" \
  "buffer_load_dwordx4 v138, s[48:51], s70 offen offset:2048 lds\n" \
  "buffer_load_dwordx4 v139, s[48:51], s70 offen offset:3072 lds\n" \
  "s_add_u32 s70, s70, 128\n" \
  "s_and_b32 s70, s70, 2047\n" \
  "s_waitcnt vmcnt(9) lgkmcnt(0)\n" \
  "ds_read_b128 a[112:115], v140 offset:49152\n" \
  "ds_read_b128 a[116:119], v141 offset:49152\n" \
  "ds_read_b128 a[120:123], v142 offset:49152\n" \
  "ds_read_b128 a[124:127], v143 offset:49152\n" \
  "s_waitcnt vmcnt(8)\n" \
  "v_cmp_ne_u32 vcc, v49, v52\n" \
  "s_cbranch_vccnz F1S_%=\n" \
  "F1K_%=:\n" \
  "s_barrier\n" \
  "s_add_u32 m0, s68, 0\n" \
  "s_add_u32 s82, s81, s80\n" \
  "buffer_load_dwordx4 v128, s[40:43], s82 offen offset:0 sc1 lds\n" \
  "s_add_u32 s80, s80, 8192\n" \
  "s_and_b32 s80, s80, 0xffff\n" \
  "buffer_load_dwordx4 v128, s[40:43], s82 offen offset:1024 sc1 lds\n" \
  "buffer_load_dwordx4 v128, s[40:43], s82 offen offset:2048 sc1 lds\n" \
  "buffer_load_dwordx4 v128, s[40:43], s82 offen offset:3072 sc1 lds\n" \
  "buffer_store_dword v135, v50, s[40:43], s69 offen sc1\n" \
  "v_and_b32 v52, 63, %[tid]\n" \
  "v_cmp_gt_u32 vcc, 12, v52\n" \
  "v_mov_b32 v49, 262144\n" \
  "s_nop 0\n" \
  "v_cndmask_b32 v52, v49, v52, vcc\n" \
  "v_cndmask_b32 v49, 0, v135, vcc\n" \
  "s_mul_i32 s60, s79, 12\n" \
  "s_mul_i32 s61, s76, 80\n" \
  "s_add_u32 s60, s60, s61\n" \
  "s_add_u32 s60, s60, 416\n" \
  "s_add_u32 m0, s68, 16384\n" \
  "s_add_u32 s82, s81, s80\n" \
  "buffer_load_dwordx4 v128, s[40:43], s82 offen offset:0 sc1 lds\n" \
  "s_add_u32 s80, s80, 8192\n" \
  "s_and_b32 s80, s80, 0xffff\n" \
  "v_add_lshl_u32 v134, v52, s60, 7\n" \
  "v_and_b32 v53, 15, %[tid]\n" \
  "v_bfe_u32 v54, %[tid], 4, 2\n" \
  "s_and_b32 s60, %[bid], 1\n" \
  "s_lshl_b32 s60, s60, 3\n" \
  "s_lshr_b32 s61, %[bid], 5\n" \
  "s_add_u32 s60, s60, s61\n" \
  "s_lshl_b32 s60, s60, 2\n" \
  "s_add_u32 s60, s60, s79\n" \
  "s_lshl_b32 s60, s60, 1\n" \
  "buffer_load_dwordx4 v128, s[40:43], s82 offen offset:1024 sc1 lds\n" \
  "s_lshr_b32 s61, s76, 1\n" \
  "s_add_u32 s60, s60, s61\n" \
  "v_lshl_add_u32 v147, s60, 4, v53\n" \
  "v_xor_b32 v148, 16, v147\n" \
  "s_lshl_b32 s60, s76, 2\n" \
  "s_add_u32 s60, s60, s77\n" \
  "s_lshl_b32 s60, s60, 2\n" \
  "s_and_b32 s61, s76, 1\n" \
  "s_lshl_b32 s62, s61, 10\n" \
  "s_lshl_b32 s61, s61, 2\n" \
  "buffer_load_dwordx4 v128, s[40:43], s82 offen offset:2048 sc1 lds\n" \
  "s_add_u32 s61, s61, s77\n" \
  "s_lshl_b32 s61, s61, 2\n" \
  "s_add_u32 s61, s61, 128\n" \
  "v_add_lshl_u32 v149, v54, s60, 4\n" \
  "v_add_lshl_u32 v151, v54, s61, 3\n" \
  "s_movk_i32 s63, 1280\n" \
  "v_mad_u32_u24 v144, v147, s63, v149\n" \
  "v_mad_u32_u24 v145, v148, s63, v149\n" \
  "v_mad_u32_u24 v146, v147, s63, v151\n" \
  "v_sub_u32 v55, v147, v53\n" \
  "buffer_load_dwordx4 v128, s[40:43], s82 offen offset:3072 sc1 lds\n" \
  "v_bfe_u32 v56, %[tid], 2, 4\n" \
  "v_add_u32 v56, v56, v55\n" \
  "v_and_b32 v57, 3, %[tid]\n" \
  "v_add_lshl_u32 v57, v57, s60, 4\n" \
  "v_mad_u32_u24 v212, v56, s63, v57\n" \
  "v_xor_b32 v56, 16, v56\n" \
  "v_mad_u32_u24 v213, v56, s63, v57\n" \
  "v_bfe_u32 v56, %[tid], 1, 4\n" \
  "v_add_u32 v56, v56, v55\n" \
  "v_and_b32 v57, 1, %[tid]\n" \
  "s_add_u32 m0, s68, 32768\n" \
  "s_add_u32 s82, s81, s80\n" \
  "buffer_load_dwordx4 v128, s[40:43], s82 offen offset:0 sc1 lds\n" \
  "s_add_u32 s80, s80, 8192\n" \
  "s_and_b32 s80, s80, 0xffff\n" \
  "s_lshr_b32 s61, s61, 1\n" \
  "v_add_lshl_u32 v57, v57, s61, 4\n" \
  "v_mad_u32_u24 v214, v56, s63, v57\n" \
  "v_lshl_add_u32 v55, v53, 2, v54\n" \
  "s_add_u32 s61, s68, 98304\n" \
  "v_lshl_add_u32 v215, v55, 4, s61\n" \
  "v_lshl_add_u32 v216, v55, 3, s61\n" \
  "v_and_b32 v55, 31, v147\n" \
  "v_lshl_add_u32 v58, v55, 3, s61\n" \
  "v_xor_b32 v59, 128, v58\n" \
  "buffer_load_dwordx4 v128, s[40:43], s82 offen offset:1024 sc1 lds\n" \
  "v_mov_b32 v218, s61\n" \
  "v_sub_u32 v55, v147, v55\n" \
  "v_lshl_add_u32 v55, v55, 3, v128\n" \
  "v_lshl_add_u32 v55, v54, 25, v55\n" \
  "v_add_u32 v217, 16384, v55\n" \
  "s_movk_i32 s61, 1024\n" \
  "v_writelane_b32 v217, s61, 16\n" \
  "v_or_b32 v130, s62, v128\n" \
  "v_xor_b32 v131, 0x400, v130\n" \
  "v_add_u32 v132, 0x10000, v130\n" \
  "buffer_load_dwordx4 v128, s[40:43], s82 offen offset:2048 sc1 lds\n" \
  "v_add_u32 v133, 0x10000, v131\n" \
  "v_lshrrev_b32 v55, 2, v53\n" \
  "v_and_b32 v56, 1, %[tid]\n" \
  "v_lshlrev_b32 v56, 4, v56\n" \
  "v_mov_b32 v57, 0x3f80\n" \
  "v_lshlrev_b32 v56, v56, v57\n" \
  "v_cmp_eq_u32 s[92:93], v55, v54\n" \
  "v_bfe_i32 v57, %[tid], 1, 1\n" \
  "s_nop 0\n" \
  "v_cndmask_b32 v56, 0, v56, s[92:93]\n" \
  "v_and_b32 v158, v56, v57\n" \
  "buffer_load_dwordx4 v128, s[40:43], s82 offen offset:3072 sc1 lds\n" \
  "buffer_load_dword v248, v51, s[48:51], s87 offen\n" \
  "v_xor_b32 v156, v56, v158\n" \
  "v_mov_b32 v157, 0\n" \
  "v_mov_b32 v159, 0\n" \
  "s_lshl_b32 s60, s68, 2\n" \
  "s_add_u32 s60, s60, 6291456\n" \
  "s_lshl_b32 s61, %[bid], 16\n" \
  "s_add_u32 s84, s60, s61\n" \
  "s_add_u32 s85, s84, 4096\n" \
  "s_add_u32 s86, s84, 8192\n" \
  "s_xor_b32 s62, s61, 0x20000\n" \
  "s_waitcnt vmcnt(10) lgkmcnt(0)\n" \
  "s_barrier\n" \
  "ds_read_b128 v[64:67], v128 offset:0\n" \
  "ds_read_b128 v[68:71], v128 offset:2048\n" \
  "ds_read_b128 v[72:75], v128 offset:4096\n" \
  "ds_read_b128 v[76:79], v128 offset:6144\n" \
  "ds_read_b128 v[80:83], v128 offset:1024\n" \
  "ds_read_b128 v[84:87], v128 offset:3072\n" \
  "ds_read_b128 v[88:91], v128 offset:5120\n" \
  "ds_read_b128 v[92:95], v128 offset:7168\n" \
  "s_waitcnt lgkmcnt(4)\n" \
  "v_mfma_f32_16x16x32_bf16 v[0:3], v[64:67], a[0:3], 0\n" \
  "ds_read_b128 v[96:99], v128 offset:8192\n" \
  "s_add_u32 s82, s81, s80\n" \
  "v_mfma_f32_16x16x32_bf16 v[0:3], v[68:71], a[8:11], v[0:3]\n" \
  "v_mfma_f32_16x16x32_bf16 v[0:3], v[72:75], a[16:19], v[0:3]\n" \
  "ds_read_b128 v[100:103], v128 offset:10240\n" \
  "s_add_u32 m0, s68, 49152\n" \
  "v_mfma_f32_16x16x32_bf16 v[0:3], v[76:79], a[24:27], v[0:3]\n" \
  "v_mfma_f32_16x16x32_bf16 v[8:11], v[64:67], a[4:7], 0\n" \
  "ds_read_b128 v[104:107], v128 offset:12288\n" \
  "buffer_load_dwordx4 v128, s[40:43], s82 offen offset:0 sc1 lds\n" \
  "v_mfma_f32_16x16x32_bf16 v[8:11], v[68:71], a[12:15], v[8:11]\n" \
  "s_add_u32 s80, s80, 8192\n" \
  "s_and_b32 s80, s80, 0xffff\n" \
  "v_mfma_f32_16x16x32_bf16 v[8:11], v[72:75], a[20:23], v[8:11]\n" \
  "buffer_load_dwordx4 v128, s[40:43], s82 offen offset:1024 sc1 lds\n" \
  "v_mfma_f32_16x16x32_bf16 v[8:11], v[76:79], a[28:31], v[8:11]\n" \
  "ds_read_b128 v[108:111], v128 offset:14336\n" \
  "s_waitcnt lgkmcnt(4)\n" \
  "v_mfma_f32_16x16x32_bf16 v[12:15], v[80:83], a[4:7], 0\n" \
  "ds_read_b128 v[112:115], v128 offset:9216\n" \
  "v_mfma_f32_16x16x32_bf16 v[12:15], v[84:87], a[12:15], v[12:15]\n" \
  "v_mfma_f32_16x16x32_bf16 v[12:15], v[88:91], a[20:23], v[12:15]\n" \
  "ds_read_b128 v[116:119], v128 offset:11264\n" \
  "buffer_load_dwordx4 v128, s[40:43], s82 offen offset:2048 sc1 lds\n" \
  "v_mfma_f32_16x16x32_bf16 v[12:15], v[92:95], a[28:31], v[12:15]\n" \
  "v_mfma_f32_16x16x32_bf16 v[4:7], v[80:83], a[0:3], 0\n" \
  "ds_read_b128 v[120:123], v128 offset:13312\n" \
  "v_mfma_f32_16x16x32_bf16 v[4:7], v[84:87], a[8:11], v[4:7]\n" \
  "v_mfma_f32_16x16x32_bf16 v[4:7], v[88:91], a[16:19], v[4:7]\n" \
  "ds_read_b128 v[124:127], v128 offset:15360\n" \
  "buffer_load_dwordx4 v128, s[40:43], s82 offen offset:3072 sc1 lds\n" \
  "v_mfma_f32_16x16x32_bf16 v[4:7], v[92:95], a[24:27], v[4:7]\n" \
  "s_waitcnt vmcnt(9) lgkmcnt(4)\n" \
  "s_barrier\n" \
  "v_mfma_f32_16x16x32_bf16 v[16:19], v[96:99], a[0:3], 0\n" \
  "ds_read_b128 v[64:67], v128 offset:16384\n" \
  "s_nop 0\n" \
  "v_mfma_f32_16x16x32_bf16 v[16:19], v[100:103], a[8:11], v[16:19]\n" \
  "s_add_u32 s88, s60, s62\n" \
  "s_xor_b32 s62, s61, 0x41000\n" \
  "v_mfma_f32_16x16x32_bf16 v[16:19], v[104:107], a[16:19], v[16:19]\n" \
  "ds_read_b128 v[68:71], v128 offset:18432\n" \
  "s_add_u32 m0, s68, 131072\n" \
  "v_mfma_f32_16x16x32_bf16 v[16:19], v[108:111], a[24:27], v[16:19]\n" \
  "s_add_u32 s89, s60, s62\n" \
  "s_xor_b32 s62, s61, 0x62000\n" \
  "v_mfma_f32_16x16x32_bf16 v[24:27], v[96:99], a[4:7], 0\n" \
  "ds_read_b128 v[72:75], v128 offset:20480\n" \
  "buffer_load_dwordx4 v136, s[48:51], s70 offen offset:0 lds\n" \
  "v_mfma_f32_16x16x32_bf16 v[24:27], v[100:103], a[12:15], v[24:27]\n" \
  "s_add_u32 s82, s81, s80\n" \
  "s_add_u32 m0, s68, 65536\n" \
  "v_mfma_f32_16x16x32_bf16 v[24:27], v[104:107], a[20:23], v[24:27]\n" \
  "buffer_load_dwordx4 v128, s[40:43], s82 offen offset:0 sc1 lds\n" \
  "s_add_u32 s80, s80, 8192\n" \
  "v_mfma_f32_16x16x32_bf16 v[24:27], v[108:111], a[28:31], v[24:27]\n" \
  "ds_read_b128 v[76:79], v128 offset:22528\n" \
  "s_waitcnt lgkmcnt(4)\n" \
  "v_mfma_f32_16x16x32_bf16 v[28:31], v[112:115], a[4:7], 0\n" \
  "ds_read_b128 v[80:83], v128 offset:17408\n" \
  "s_and_b32 s80, s80, 0xffff\n" \
  "v_mfma_f32_16x16x32_bf16 v[28:31], v[116:119], a[12:15], v[28:31]\n" \
  "s_add_u32 s90, s60, s62\n" \
  "v_and_b32 v155, 63, %[tid]\n" \
  "v_mfma_f32_16x16x32_bf16 v[28:31], v[120:123], a[20:23], v[28:31]\n" \
  "ds_read_b128 v[84:87], v128 offset:19456\n" \
  "s_add_u32 m0, s68, 131072\n" \
  "v_mfma_f32_16x16x32_bf16 v[28:31], v[124:127], a[28:31], v[28:31]\n" \
  "v_min_u32 v155, 1, v155\n" \
  "v_lshlrev_b32 v155, 25, v155\n" \
  "v_mfma_f32_16x16x32_bf16 v[20:23], v[112:115], a[0:3], 0\n" \
  "ds_read_b128 v[88:91], v128 offset:21504\n" \
  "buffer_load_dwordx4 v137, s[48:51], s70 offen offset:1024 lds\n" \
  "v_mfma_f32_16x16x32_bf16 v[20:23], v[116:119], a[8:11], v[20:23]\n" \
  "buffer_load_dwordx4 v138, s[48:51], s70 offen offset:2048 lds\n" \
  "s_mov_b64 s[72:73], 1\n" \
  "v_mfma_f32_16x16x32_bf16 v[20:23], v[120:123], a[16:19], v[20:23]\n" \
  "s_add_u32 m0, s68, 65536\n" \
  "v_mov_b32 v37, s71\n" \
  "v_mfma_f32_16x16x32_bf16 v[20:23], v[124:127], a[24:27], v[20:23]\n" \
  "ds_read_b128 v[92:95], v128 offset:23552\n" \
  "s_waitcnt lgkmcnt(4)\n" \
  "v_mfma_f32_16x16x32_bf16 v[0:3], v[64:67], a[32:35], v[0:3]\n" \
  "ds_read_b128 v[96:99], v128 offset:24576\n" \
  "buffer_load_dwordx4 v128, s[40:43], s82 offen offset:1024 sc1 lds\n" \
  "v_mfma_f32_16x16x32_bf16 v[0:3], v[68:71], a[40:43], v[0:3]\n" \
  "v_mov_b32 v39, s71\n" \
  "v_mov_b32 v41, s71\n" \
  "v_mfma_f32_16x16x32_bf16 v[0:3], v[72:75], a[48:51], v[0:3]\n" \
  "ds_read_b128 v[100:103], v128 offset:26624\n" \
  "s_add_u32 m0, s68, 131072\n" \
  "v_mfma_f32_16x16x32_bf16 v[0:3], v[76:79], a[56:59], v[0:3]\n" \
  "v_mov_b32 v43, s71\n" \
  "v_mov_b32 v45, s71\n" \
  "v_mfma_f32_16x16x32_bf16 v[8:11], v[64:67], a[36:39], v[8:11]\n" \
  "ds_read_b128 v[104:107], v128 offset:28672\n" \
  "buffer_load_dwordx4 v139, s[48:51], s70 offen offset:3072 lds\n" \
  "v_mfma_f32_16x16x32_bf16 v[8:11], v[68:71], a[44:47], v[8:11]\n" \
  "s_add_u32 s70, s70, 128\n" \
  "s_and_b32 s70, s70, 2047\n" \
  "v_mfma_f32_16x16x32_bf16 v[8:11], v[72:75], a[52:55], v[8:11]\n" \
  "s_add_u32 m0, s68, 65536\n" \
  "v_mov_b32 v47, s71\n" \
  "v_mfma_f32_16x16x32_bf16 v[8:11], v[76:79], a[60:63], v[8:11]\n" \
  "ds_read_b128 v[108:111], v128 offset:30720\n" \
  "s_waitcnt lgkmcnt(4)\n" \
  "v_mfma_f32_16x16x32_bf16 v[12:15], v[80:83], a[36:39], v[12:15]\n" \
  "ds_read_b128 v[112:115], v128 offset:25600\n" \
  "buffer_load_dwordx4 v128, s[40:43], s82 offen offset:2048 sc1 lds\n" \
  "v_mfma_f32_16x16x32_bf16 v[12:15], v[84:87], a[44:47], v[12:15]\n" \
  "s_and_b32 s60, %[bid], 7\n" \
  "s_mul_i32 s60, s60, 80\n" \
  "v_mfma_f32_16x16x32_bf16 v[12:15], v[88:91], a[52:55], v[12:15]\n" \
  "PAD3_%=:\n" \
  "s_cbranch_execz PAD7_%=\n" \
  "ds_read_b128 v[116:119], v128 offset:27648\n" \
  "s_lshr_b32 s61, %[bid], 3\n" \
  "v_mfma_f32_16x16x32_bf16 v[12:15], v[92:95], a[60:63], v[12:15]\n" \
  "s_lshl_b32 s61, s61, 2\n" \
  "s_add_u32 s61, s61, s79\n" \
  "v_mfma_f32_16x16x32_bf16 v[4:7], v[80:83], a[32:35], v[4:7]\n" \
  "ds_read_b128 v[120:123], v128 offset:29696\n" \
  "buffer_load_dwordx4 v128, s[40:43], s82 offen offset:3072 sc1 lds\n" \
  "v_mfma_f32_16x16x32_bf16 v[4:7], v[84:87], a[40:43], v[4:7]\n" \
  "s_add_u32 s60, s60, s61\n" \
  "v_mfma_f32_16x16x32_bf16 v[4:7], v[88:91], a[48:51], v[4:7]\n" \
  "ds_read_b128 v[124:127], v128 offset:31744\n" \
  "s_nop 0\n" \
  "v_mfma_f32_16x16x32_bf16 v[4:7], v[92:95], a[56:59], v[4:7]\n" \
  "s_waitcnt vmcnt(13) lgkmcnt(4)\n" \
  "s_barrier\n" \
  "v_mfma_f32_16x16x32_bf16 v[16:19], v[96:99], a[32:35], v[16:19]\n" \
  "ds_read_b128 v[64:67], v128 offset:32768\n" \
  "s_add_u32 m0, s68, 147456\n" \
  "v_mfma_f32_16x16x32_bf16 v[16:19], v[100:103], a[40:43], v[16:19]\n" \
  "s_cmp_lt_u32 s61, 80\n" \
  "s_cselect_b32 s61, 28, 0\n" \
  "v_mfma_f32_16x16x32_bf16 v[16:19], v[104:107], a[48:51], v[16:19]\n" \
  "ds_read_b128 v[68:71], v128 offset:34816\n" \
  "buffer_load_dwordx4 v136, s[48:51], s70 offen offset:0 lds\n" \
  "v_mfma_f32_16x16x32_bf16 v[16:19], v[108:111], a[56:59], v[16:19]\n" \
  "v_and_b32 v55, 63, %[tid]\n" \
  "v_cmp_gt_u32 s[92:93], s61, v55\n" \
  "v_mfma_f32_16x16x32_bf16 v[24:27], v[96:99], a[36:39], v[24:27]\n" \
  "ds_read_b128 v[72:75], v128 offset:36864\n" \
  "buffer_load_dwordx4 v137, s[48:51], s70 offen offset:1024 lds\n" \
  "v_mfma_f32_16x16x32_bf16 v[24:27], v[100:103], a[44:47], v[24:27]\n" \
  "s_add_u32 s82, s81, s80\n" \
  "s_add_u32 m0, s68, 81920\n" \
  "v_mfma_f32_16x16x32_bf16 v[24:27], v[104:107], a[52:55], v[24:27]\n" \
  "buffer_load_dwordx4 v128, s[40:43], s82 offen offset:0 sc1 lds\n" \
  "s_add_u32 s80, s80, 8192\n" \
  "v_mfma_f32_16x16x32_bf16 v[24:27], v[108:111], a[60:63], v[24:27]\n" \
  "ds_read_b128 v[76:79], v128 offset:38912\n" \
  "s_waitcnt lgkmcnt(4)\n" \
  "v_mfma_f32_16x16x32_bf16 v[28:31], v[112:115], a[36:39], v[28:31]\n" \
  "ds_read_b128 v[80:83], v128 offset:33792\n" \
  "s_and_b32 s80, s80, 0xffff\n" \
  "v_mfma_f32_16x16x32_bf16 v[28:31], v[116:119], a[44:47], v[28:31]\n" \
  "s_nop 0\n" \
  "ds_read_b128 a[128:131], v140 offset:0\n" \
  "v_mfma_f32_16x16x32_bf16 v[28:31], v[120:123], a[52:55], v[28:31]\n" \
  "ds_read_b128 v[84:87], v128 offset:35840\n" \
  "s_add_u32 m0, s68, 147456\n" \
  "v_mfma_f32_16x16x32_bf16 v[28:31], v[124:127], a[60:63], v[28:31]\n" \
  "ds_read_b128 a[132:135], v141 offset:0\n" \
  "buffer_load_dwordx4 v138, s[48:51], s70 offen offset:2048 lds\n" \
  "v_mfma_f32_16x16x32_bf16 v[20:23], v[112:115], a[32:35], v[20:23]\n" \
  "ds_read_b128 v[88:91], v128 offset:37888\n" \
  "buffer_load_dwordx4 v139, s[48:51], s70 offen offset:3072 lds\n" \
  "v_mfma_f32_16x16x32_bf16 v[20:23], v[116:119], a[40:43], v[20:23]\n" \
  "ds_read_b128 a[136:139], v142 offset:0\n" \
  "s_add_u32 s70, s70, 128\n" \
  "v_mfma_f32_16x16x32_bf16 v[20:23], v[120:123], a[48:51], v[20:23]\n" \
  "ds_read_b128 a[140:143], v143 offset:0\n" \
  "s_and_b32 s70, s70, 2047\n" \
  "v_mfma_f32_16x16x32_bf16 v[20:23], v[124:127], a[56:59], v[20:23]\n" \
  "ds_read_b128 v[92:95], v128 offset:39936\n" \
  "s_waitcnt lgkmcnt(8)\n" \
  "v_mfma_f32_16x16x32_bf16 v[0:3], v[64:67], a[64:67], v[0:3]\n" \
  "ds_read_b128 v[96:99], v128 offset:40960\n" \
  "s_add_u32 m0, s68, 81920\n" \
  "v_mfma_f32_16x16x32_bf16 v[0:3], v[68:71], a[72:75], v[0:3]\n" \
  "s_min_u32 s61, s61, 1\n" \
  "v_cmp_gt_u32 s[94:95], s61, v55\n" \
  "v_mfma_f32_16x16x32_bf16 v[0:3], v[72:75], a[80:83], v[0:3]\n" \
  "ds_read_b128 v[100:103], v128 offset:43008\n" \
  "buffer_load_dwordx4 v128, s[40:43], s82 offen offset:1024 sc1 lds\n" \
  "v_mfma_f32_16x16x32_bf16 v[0:3], v[76:79], a[88:91], v[0:3]\n" \
  "s_lshl_b32 s62, s60, 4\n" \
  "s_add_u32 s62, s62, 131072\n" \
  "v_mfma_f32_16x16x32_bf16 v[8:11], v[64:67], a[68:71], v[8:11]\n" \
  "ds_read_b128 v[104:107], v128 offset:45056\n" \
  "buffer_load_dwordx4 v128, s[40:43], s82 offen offset:2048 sc1 lds\n" \
  "v_mfma_f32_16x16x32_bf16 v[8:11], v[68:71], a[76:79], v[8:11]\n" \
  "s_waitcnt lgkmcnt(4)\n" \
  "s_add_u32 m0, s68, 98304\n" \
  "v_mfma_f32_16x16x32_bf16 v[8:11], v[72:75], a[84:87], v[8:11]\n" \
  "buffer_load_dwordx4 v136, s[48:51], s70 offen offset:0 lds\n" \
  "v_mul_u32_u24 v55, 10240, v55\n" \
  "v_mfma_f32_16x16x32_bf16 v[8:11], v[76:79], a[92:95], v[8:11]\n" \
  "ds_read_b128 v[108:111], v128 offset:47104\n" \
  "s_waitcnt lgkmcnt(4)\n" \
  "v_mfma_f32_16x16x32_bf16 v[12:15], v[80:83], a[68:71], v[12:15]\n" \
  "ds_read_b128 v[112:115], v128 offset:41984\n" \
  "v_add_u32 v55, s62, v55\n" \
  "v_mfma_f32_16x16x32_bf16 v[12:15], v[84:87], a[76:79], v[12:15]\n" \
  "v_mov_b32 v56, 33554432\n" \
  "v_cndmask_b32 v152, v56, v55, s[92:93]\n" \
  "v_mfma_f32_16x16x32_bf16 v[12:15], v[88:91], a[84:87], v[12:15]\n" \
  "ds_read_b128 v[116:119], v128 offset:44032\n" \
  "buffer_load_dwordx4 v137, s[48:51], s70 offen offset:1024 lds\n" \
  "v_mfma_f32_16x16x32_bf16 v[12:15], v[92:95], a[92:95], v[12:15]\n" \
  "v_cndmask_b32 v154, 0, -1, s[92:93]\n" \
  "s_lshl_b32 s62, s60, 3\n" \
  "v_mfma_f32_16x16x32_bf16 v[4:7], v[80:83], a[64:67], v[4:7]\n" \
  "ds_read_b128 v[120:123], v128 offset:46080\n" \
  "s_add_u32 m0, s68, 81920\n" \
  "v_mfma_f32_16x16x32_bf16 v[4:7], v[84:87], a[72:75], v[4:7]\n" \
  "s_add_u32 s62, s62, 2048\n" \
  "v_mov_b32 v55, s62\n" \
  "v_mfma_f32_16x16x32_bf16 v[4:7], v[88:91], a[80:83], v[4:7]\n" \
  "ds_read_b128 v[124:127], v128 offset:48128\n" \
  "buffer_load_dwordx4 v128, s[40:43], s82 offen offset:3072 sc1 lds\n" \
  "v_mfma_f32_16x16x32_bf16 v[4:7], v[92:95], a[88:91], v[4:7]\n" \
  "s_waitcnt vmcnt(18) lgkmcnt(4)\n" \
  "s_barrier\n" \
  "v_mfma_f32_16x16x32_bf16 v[16:19], v[96:99], a[64:67], v[16:19]\n" \
  "ds_read_b128 v[64:67], v128 offset:49152\n" \
  "s_add_u32 m0, s68, 98304\n" \
  "v_mfma_f32_16x16x32_bf16 v[16:19], v[100:103], a[72:75], v[16:19]\n" \
  "v_cndmask_b32 v153, v56, v55, s[94:95]\n" \
  "v_mfma_f32_16x16x32_bf16 v[16:19], v[104:107], a[80:83], v[16:19]\n" \
  "ds_read_b128 v[68:71], v128 offset:51200\n" \
  "buffer_load_dwordx4 v138, s[48:51], s70 offen offset:2048 lds\n" \
  "v_mfma_f32_16x16x32_bf16 v[16:19], v[108:111], a[88:91], v[16:19]\n" \
  "s_nop 0\n" \
  "ds_read_b128 a[144:147], v140 offset:16384\n" \
  "v_mfma_f32_16x16x32_bf16 v[24:27], v[96:99], a[68:71], v[24:27]\n" \
  "ds_read_b128 v[72:75], v128 offset:53248\n" \
  "buffer_load_dwordx4 v139, s[48:51], s70 offen offset:3072 lds\n" \
  "v_mfma_f32_16x16x32_bf16 v[24:27], v[100:103], a[76:79], v[24:27]\n" \
  "ds_read_b128 a[148:151], v141 offset:16384\n" \
  "s_add_u32 s70, s70, 128\n" \
  "v_mfma_f32_16x16x32_bf16 v[24:27], v[104:107], a[84:87], v[24:27]\n" \
  "ds_read_b128 a[152:155], v142 offset:16384\n" \
  "s_and_b32 s70, s70, 2047\n" \
  "v_mfma_f32_16x16x32_bf16 v[24:27], v[108:111], a[92:95], v[24:27]\n" \
  "ds_read_b128 v[76:79], v128 offset:55296\n" \
  "s_waitcnt lgkmcnt(7)\n" \
  "v_mfma_f32_16x16x32_bf16 v[28:31], v[112:115], a[68:71], v[28:31]\n" \
  "ds_read_b128 v[80:83], v128 offset:50176\n" \
  "s_add_u32 s82, s81, s80\n" \
  "v_mfma_f32_16x16x32_bf16 v[28:31], v[116:119], a[76:79], v[28:31]\n" \
  "ds_read_b128 a[156:159], v143 offset:16384\n" \
  "s_add_u32 m0, s68, 0\n" \
  "v_mfma_f32_16x16x32_bf16 v[28:31], v[120:123], a[84:87], v[28:31]\n" \
  "ds_read_b128 v[84:87], v128 offset:52224\n" \
  "buffer_load_dwordx4 v128, s[40:43], s82 offen offset:0 sc1 lds\n" \
  "v_mfma_f32_16x16x32_bf16 v[28:31], v[124:127], a[92:95], v[28:31]\n" \
  "v_mfma_f32_16x16x32_bf16 v[20:23], v[112:115], a[64:67], v[20:23]\n" \
  "ds_read_b128 v[88:91], v128 offset:54272\n" \
  "s_add_u32 s80, s80, 8192\n" \
  "v_mfma_f32_16x16x32_bf16 v[20:23], v[116:119], a[72:75], v[20:23]\n" \
  "s_and_b32 s80, s80, 0xffff\n" \
  "buffer_load_dwordx4 v128, s[40:43], s82 offen offset:1024 sc1 lds\n" \
  "v_mfma_f32_16x16x32_bf16 v[20:23], v[120:123], a[80:83], v[20:23]\n" \
  "s_waitcnt lgkmcnt(2)\n" \
  "s_add_u32 m0, s68, 114688\n" \
  "v_mfma_f32_16x16x32_bf16 v[20:23], v[124:127], a[88:91], v[20:23]\n" \
  "ds_read_b128 v[92:95], v128 offset:56320\n" \
  "s_nop 0\n" \
  "v_mfma_f32_16x16x32_bf16 v[0:3], v[64:67], a[96:99], v[0:3]\n" \
  "ds_read_b128 v[96:99], v128 offset:57344\n" \
  "buffer_load_dwordx4 v136, s[48:51], s70 offen offset:0 lds\n" \
  "v_mfma_f32_16x16x32_bf16 v[0:3], v[68:71], a[104:107], v[0:3]\n" \
  "v_mfma_f32_16x16x32_bf16 v[0:3], v[72:75], a[112:115], v[0:3]\n" \
  "ds_read_b128 v[100:103], v128 offset:59392\n" \
  "s_add_u32 m0, s68, 0\n" \
  "v_mfma_f32_16x16x32_bf16 v[0:3], v[76:79], a[120:123], v[0:3]\n" \
  "v_mfma_f32_16x16x32_bf16 v[8:11], v[64:67], a[100:103], v[8:11]\n" \
  "ds_read_b128 v[104:107], v128 offset:61440\n" \
  "buffer_load_dwordx4 v128, s[40:43], s82 offen offset:2048 sc1 lds\n" \
  "v_mfma_f32_16x16x32_bf16 v[8:11], v[68:71], a[108:111], v[8:11]\n" \
  "s_add_u32 m0, s68, 114688\n" \
  "v_mfma_f32_16x16x32_bf16 v[8:11], v[72:75], a[116:119], v[8:11]\n" \
  "buffer_load_dwordx4 v137, s[48:51], s70 offen offset:1024 lds\n" \
  "v_mfma_f32_16x16x32_bf16 v[8:11], v[76:79], a[124:127], v[8:11]\n" \
  "ds_read_b128 v[108:111], v128 offset:63488\n" \
  "s_waitcnt lgkmcnt(4)\n" \
  "v_mfma_f32_16x16x32_bf16 v[12:15], v[80:83], a[100:103], v[12:15]\n" \
  "ds_read_b128 v[112:115], v128 offset:58368\n" \
  "buffer_load_dwordx4 v138, s[48:51], s70 offen offset:2048 lds\n" \
  "v_mfma_f32_16x16x32_bf16 v[12:15], v[84:87], a[108:111], v[12:15]\n" \
  "v_mfma_f32_16x16x32_bf16 v[12:15], v[88:91], a[116:119], v[12:15]\n" \
  "ds_read_b128 v[116:119], v128 offset:60416\n" \
  "s_add_u32 m0, s68, 0\n" \
  "v_mfma_f32_16x16x32_bf16 v[12:15], v[92:95], a[124:127], v[12:15]\n" \
  "v_mfma_f32_16x16x32_bf16 v[4:7], v[80:83], a[96:99], v[4:7]\n" \
  "ds_read_b128 v[120:123], v128 offset:62464\n" \
  "buffer_load_dwordx4 v128, s[40:43], s82 offen offset:3072 sc1 lds\n" \
  "v_mfma_f32_16x16x32_bf16 v[4:7], v[84:87], a[104:107], v[4:7]\n" \
  "s_waitcnt vmcnt(21)\n" \
  "ds_read_b128 a[160:163], v140 offset:32768\n" \
  "v_mfma_f32_16x16x32_bf16 v[4:7], v[88:91], a[112:115], v[4:7]\n" \
  "ds_read_b128 v[124:127], v128 offset:64512\n" \
  "s_add_u32 m0, s68, 114688\n" \
  "v_mfma_f32_16x16x32_bf16 v[4:7], v[92:95], a[120:123], v[4:7]\n" \
  "s_waitcnt vmcnt(19) lgkmcnt(5)\n" \
  "s_barrier\n" \
  "v_mfma_f32_16x16x32_bf16 v[16:19], v[96:99], a[96:99], v[16:19]\n" \
  "ds_read_b128 v[64:67], v129 offset:0\n" \
  "buffer_load_dwordx4 v139, s[48:51], s70 offen offset:3072 lds\n" \
  "v_mfma_f32_16x16x32_bf16 v[16:19], v[100:103], a[104:107], v[16:19]\n" \
  "ds_read_b128 a[164:167], v141 offset:32768\n" \
  "s_add_u32 s70, s70, 128\n" \
  "v_mfma_f32_16x16x32_bf16 v[16:19], v[104:107], a[112:115], v[16:19]\n" \
  "ds_read_b128 v[68:71], v129 offset:2048\n" \
  "s_and_b32 s70, s70, 2047\n" \
  "v_mfma_f32_16x16x32_bf16 v[16:19], v[108:111], a[120:123], v[16:19]\n" \
  "ds_read_b128 a[168:171], v142 offset:32768\n" \
  "s_add_u32 s82, s81, s80\n" \
  "v_mfma_f32_16x16x32_bf16 v[24:27], v[96:99], a[100:103], v[24:27]\n" \
  "ds_read_b128 v[72:75], v129 offset:4096\n" \
  "s_add_u32 m0, s68, 16384\n" \
  "v_mfma_f32_16x16x32_bf16 v[24:27], v[100:103], a[108:111], v[24:27]\n" \
  "ds_read_b128 a[172:175], v143 offset:32768\n" \
  "buffer_load_dwordx4 v128, s[40:43], s82 offen offset:0 sc1 lds\n" \
  "v_mfma_f32_16x16x32_bf16 v[24:27], v[104:107], a[116:119], v[24:27]\n" \
  "s_add_u32 s80, s83, s68\n" \
  "s_and_b32 s80, s80, 0xffff\n" \
  "v_mfma_f32_16x16x32_bf16 v[24:27], v[108:111], a[124:127], v[24:27]\n" \
  "ds_read_b128 v[76:79], v129 offset:6144\n" \
  "s_waitcnt lgkmcnt(7)\n" \
  "v_mfma_f32_16x16x32_bf16 v[28:31], v[112:115], a[100:103], v[28:31]\n" \
  "ds_read_b128 v[80:83], v129 offset:1024\n" \
  "s_mov_b32 s81, s75\n" \
  "v_mfma_f32_16x16x32_bf16 v[28:31], v[116:119], a[108:111], v[28:31]\n" \
  "v_mfma_f32_16x16x32_bf16 v[28:31], v[120:123], a[116:119], v[28:31]\n" \
  "ds_read_b128 v[84:87], v129 offset:3072\n" \
  "s_waitcnt lgkmcnt(3)\n" \
  "v_mfma_f32_16x16x32_bf16 v[28:31], v[124:127], a[124:127], v[28:31]\n" \
  "v_mfma_f32_16x16x32_bf16 v[20:23], v[112:115], a[96:99], v[20:23]\n" \
  "ds_read_b128 v[88:91], v129 offset:5120\n" \
  "s_add_u32 m0, s68, 131072\n" \
  "v_mfma_f32_16x16x32_bf16 v[20:23], v[116:119], a[104:107], v[20:23]\n" \
  "buffer_load_dwordx4 v136, s[48:51], s70 offen offset:0 lds\n" \
  "v_mfma_f32_16x16x32_bf16 v[20:23], v[120:123], a[112:115], v[20:23]\n" \
  "buffer_load_dword v52, v134, s[40:43], 0 offen sc1\n" \
  "s_add_u32 m0, s68, 16384\n" \
  "v_mfma_f32_16x16x32_bf16 v[20:23], v[124:127], a[120:123], v[20:23]\n" \
  "ds_read_b128 v[92:95], v129 offset:7168\n" \
  "s_waitcnt lgkmcnt(4)\n" \
  "v_mfma_f32_16x16x32_bf16 v[0:3], v[64:67], a[128:131], v[0:3]\n" \
  "ds_read_b128 v[96:99], v129 offset:8192\n" \
  "buffer_load_dwordx4 v128, s[40:43], s82 offen offset:1024 sc1 lds\n" \
  "v_mfma_f32_16x16x32_bf16 v[0:3], v[68:71], a[136:139], v[0:3]\n" \
  "v_mfma_f32_16x16x32_bf16 v[0:3], v[72:75], a[144:147], v[0:3]\n" \
  "ds_read_b128 v[100:103], v129 offset:10240\n" \
  "s_add_u32 m0, s68, 131072\n" \
  "v_mfma_f32_16x16x32_bf16 v[0:3], v[76:79], a[152:155], v[0:3]\n" \
  "v_mfma_f32_16x16x32_bf16 v[8:11], v[64:67], a[132:135], v[8:11]\n" \
  "ds_read_b128 v[104:107], v129 offset:12288\n" \
  "buffer_load_dwordx4 v137, s[48:51], s70 offen offset:1024 lds\n" \
  "v_mfma_f32_16x16x32_bf16 v[8:11], v[68:71], a[140:143], v[8:11]\n" \
  "buffer_load_dwordx4 v138, s[48:51], s70 offen offset:2048 lds\n" \
  "v_mfma_f32_16x16x32_bf16 v[8:11], v[72:75], a[148:151], v[8:11]\n" \
  "s_waitcnt vmcnt(21)\n" \
  "ds_read_b128 a[176:179], v140 offset:49152\n" \
  "v_mfma_f32_16x16x32_bf16 v[8:11], v[76:79], a[156:159], v[8:11]\n" \
  "ds_read_b128 v[108:111], v129 offset:14336\n" \
  "s_waitcnt lgkmcnt(5)\n" \
  "v_mfma_f32_16x16x32_bf16 v[12:15], v[80:83], a[132:135], v[12:15]\n" \
  "ds_read_b128 v[112:115], v129 offset:9216\n" \
  "s_add_u32 m0, s68, 16384\n" \
  "v_mfma_f32_16x16x32_bf16 v[12:15], v[84:87], a[140:143], v[12:15]\n" \
  "ds_read_b128 a[180:183], v141 offset:49152\n" \
  "buffer_load_dwordx4 v128, s[40:43], s82 offen offset:2048 sc1 lds\n" \
  "v_mfma_f32_16x16x32_bf16 v[12:15], v[88:91], a[148:151], v[12:15]\n" \
  "ds_read_b128 v[116:119], v129 offset:11264\n" \
  "s_add_u32 m0, s68, 131072\n" \
  "v_mfma_f32_16x16x32_bf16 v[12:15], v[92:95], a[156:159], v[12:15]\n" \
  "ds_read_b128 a[184:187], v142 offset:49152\n" \
  "buffer_load_dwordx4 v139, s[48:51], s70 offen offset:3072 lds\n" \
  "v_mfma_f32_16x16x32_bf16 v[4:7], v[80:83], a[128:131], v[4:7]\n" \
  "ds_read_b128 v[120:123], v129 offset:13312\n" \
  "s_add_u32 s70, s70, 128\n" \
  "v_mfma_f32_16x16x32_bf16 v[4:7], v[84:87], a[136:139], v[4:7]\n" \
  "ds_read_b128 a[188:191], v143 offset:49152\n" \
  "s_and_b32 s70, s70, 2047\n" \
  "v_mfma_f32_16x16x32_bf16 v[4:7], v[88:91], a[144:147], v[4:7]\n" \
  "ds_read_b128 v[124:127], v129 offset:15360\n" \
  "s_add_u32 m0, s68, 16384\n" \
  "v_mfma_f32_16x16x32_bf16 v[4:7], v[92:95], a[152:155], v[4:7]\n" \
  "s_waitcnt vmcnt(18) lgkmcnt(7)\n" \
  "s_barrier\n" \
  "v_mfma_f32_16x16x32_bf16 v[16:19], v[96:99], a[128:131], v[16:19]\n" \
  "ds_read_b128 v[64:67], v129 offset:16384\n" \
  "buffer_load_dwordx4 v128, s[40:43], s82 offen offset:3072 sc1 lds\n" \
  "v_mfma_f32_16x16x32_bf16 v[16:19], v[100:103], a[136:139], v[16:19]\n" \
  "v_mfma_f32_16x16x32_bf16 v[16:19], v[104:107], a[144:147], v[16:19]\n" \
  "ds_read_b128 v[68:71], v129 offset:18432\n" \
  "s_waitcnt lgkmcnt(3)\n" \
  "v_mfma_f32_16x16x32_bf16 v[16:19], v[108:111], a[152:155], v[16:19]\n" \
  "v_mfma_f32_16x16x32_bf16 v[24:27], v[96:99], a[132:135], v[24:27]\n" \
  "ds_read_b128 v[72:75], v129 offset:20480\n" \
  "s_add_u32 m0, s68, 147456\n" \
  "v_mfma_f32_16x16x32_bf16 v[24:27], v[100:103], a[140:143], v[24:27]\n" \
  "buffer_load_dwordx4 v136, s[48:51], s70 offen offset:0 lds\n" \
  "v_mfma_f32_16x16x32_bf16 v[24:27], v[104:107], a[148:151], v[24:27]\n" \
  "v_mfma_f32_16x16x32_bf16 v[24:27], v[108:111], a[156:159], v[24:27]\n" \
  "ds_read_b128 v[76:79], v129 offset:22528\n" \
  "s_waitcnt lgkmcnt(4)\n" \
  "v_mfma_f32_16x16x32_bf16 v[28:31], v[112:115], a[132:135], v[28:31]\n" \
  "ds_read_b128 v[80:83], v129 offset:17408\n" \
  "buffer_load_dwordx4 v137, s[48:51], s70 offen offset:1024 lds\n" \
  "v_mfma_f32_16x16x32_bf16 v[28:31], v[116:119], a[140:143], v[28:31]\n" \
  "v_mfma_f32_16x16x32_bf16 v[28:31], v[120:123], a[148:151], v[28:31]\n" \
  "ds_read_b128 v[84:87], v129 offset:19456\n" \
  "v_mfma_f32_16x16x32_bf16 v[28:31], v[124:127], a[156:159], v[28:31]\n" \
  "v_mfma_f32_16x16x32_bf16 v[20:23], v[112:115], a[128:131], v[20:23]\n" \
  "ds_read_b128 v[88:91], v129 offset:21504\n" \
  "buffer_load_dwordx4 v138, s[48:51], s70 offen offset:2048 lds\n" \
  "v_mfma_f32_16x16x32_bf16 v[20:23], v[116:119], a[136:139], v[20:23]\n" \
  "v_mfma_f32_16x16x32_bf16 v[20:23], v[120:123], a[144:147], v[20:23]\n" \
  "v_mfma_f32_16x16x32_bf16 v[20:23], v[124:127], a[152:155], v[20:23]\n" \
  "ds_read_b128 v[92:95], v129 offset:23552\n" \
  "s_waitcnt lgkmcnt(4)\n" \
  "v_mfma_f32_16x16x32_bf16 v[0:3], v[64:67], a[160:163], v[0:3]\n" \
  "ds_read_b128 v[96:99], v129 offset:24576\n" \
  "buffer_load_dwordx4 v139, s[48:51], s70 offen offset:3072 lds\n" \
  "v_mfma_f32_16x16x32_bf16 v[0:3], v[68:71], a[168:171], v[0:3]\n" \
  "s_waitcnt vmcnt(21)\n" \
  "ds_read_b128 a[192:195], v140 offset:0\n" \
  "v_mfma_f32_16x16x32_bf16 v[0:3], v[72:75], a[176:179], v[0:3]\n" \
  "ds_read_b128 v[100:103], v129 offset:26624\n" \
  "s_add_u32 s70, s70, 128\n" \
  "v_mfma_f32_16x16x32_bf16 v[0:3], v[76:79], a[184:187], v[0:3]\n" \
  "ds_read_b128 a[196:199], v141 offset:0\n" \
  "v_mfma_f32_16x16x32_bf16 v[8:11], v[64:67], a[164:167], v[8:11]\n" \
  "ds_read_b128 v[104:107], v129 offset:28672\n" \
  "s_and_b32 s70, s70, 2047\n" \
  "v_mfma_f32_16x16x32_bf16 v[8:11], v[68:71], a[172:175], v[8:11]\n" \
  "ds_read_b128 a[200:203], v142 offset:0\n" \
  "v_mfma_f32_16x16x32_bf16 v[8:11], v[72:75], a[180:183], v[8:11]\n" \
  "ds_read_b128 a[204:207], v143 offset:0\n" \
  "v_mfma_f32_16x16x32_bf16 v[8:11], v[76:79], a[188:191], v[8:11]\n" \
  "ds_read_b128 v[108:111], v129 offset:30720\n" \
  "s_waitcnt lgkmcnt(8)\n" \
  "v_mfma_f32_16x16x32_bf16 v[12:15], v[80:83], a[164:167], v[12:15]\n" \
  "ds_read_b128 v[112:115], v129 offset:25600\n" \
  "v_mfma_f32_16x16x32_bf16 v[12:15], v[84:87], a[172:175], v[12:15]\n" \
  "v_mfma_f32_16x16x32_bf16 v[12:15], v[88:91], a[180:183], v[12:15]\n" \
  "ds_read_b128 v[116:119], v129 offset:27648\n" \
  "s_waitcnt lgkmcnt(3)\n" \
  "v_mfma_f32_16x16x32_bf16 v[12:15], v[92:95], a[188:191], v[12:15]\n" \
  "s_waitcnt vmcnt(10)\n" \
  "s_add_u32 m0, s68, 98304\n" \
  "v_mfma_f32_16x16x32_bf16 v[4:7], v[80:83], a[160:163], v[4:7]\n" \
  "ds_read_b128 v[120:123], v129 offset:29696\n" \
  "buffer_load_dwordx4 v212, s[36:39], 0 offen lds\n" \
  "v_mfma_f32_16x16x32_bf16 v[4:7], v[84:87], a[168:171], v[4:7]\n" \
  "v_cmp_ne_u32 vcc, v49, v52\n" \
  "s_cbranch_vccnz F2S_%=\n" \
  "F2K_%=:\n" \
  "v_mfma_f32_16x16x32_bf16 v[4:7], v[88:91], a[176:179], v[4:7]\n" \
  "ds_read_b128 v[124:127], v129 offset:31744\n" \
  "s_add_u32 m0, s68, 99328\n" \
  "v_mfma_f32_16x16x32_bf16 v[4:7], v[92:95], a[184:187], v[4:7]\n" \
  "s_waitcnt lgkmcnt(4)\n" \
  "s_barrier\n" \
  "v_mfma_f32_16x16x32_bf16 v[16:19], v[96:99], a[160:163], v[16:19]\n" \
  "ds_read_b128 v[64:67], v128 offset:0\n" \
  "buffer_load_dwordx4 v213, s[36:39], 0 offen lds\n" \
  "v_mfma_f32_16x16x32_bf16 v[16:19], v[100:103], a[168:171], v[16:19]\n" \
  "s_nop 0\n" \
  "ds_read_b128 a[208:211], v140 offset:16384\n" \
  "v_mfma_f32_16x16x32_bf16 v[16:19], v[104:107], a[176:179], v[16:19]\n" \
  "ds_read_b128 v[68:71], v128 offset:2048\n" \
  "s_add_u32 m0, s68, 100352\n" \
  "v_mfma_f32_16x16x32_bf16 v[16:19], v[108:111], a[184:187], v[16:19]\n" \
  "ds_read_b128 a[212:215], v141 offset:16384\n" \
  "buffer_load_dwordx4 v214, s[36:39], 0 offen lds\n" \
  "v_mfma_f32_16x16x32_bf16 v[24:27], v[96:99], a[164:167], v[24:27]\n" \
  "ds_read_b128 v[72:75], v128 offset:4096\n" \
  "s_add_u32 s82, s81, s80\n" \
  "v_mfma_f32_16x16x32_bf16 v[24:27], v[100:103], a[172:175], v[24:27]\n" \
  "ds_read_b128 a[216:219], v142 offset:16384\n" \
  "s_add_u32 m0, s68, 32768\n" \
  "v_mfma_f32_16x16x32_bf16 v[24:27], v[104:107], a[180:183], v[24:27]\n" \
  "ds_read_b128 a[220:223], v143 offset:16384\n" \
  "buffer_load_dwordx4 v128, s[40:43], s82 offen offset:0 sc1 lds\n" \
  "v_mfma_f32_16x16x32_bf16 v[24:27], v[108:111], a[188:191], v[24:27]\n" \
  "ds_read_b128 v[76:79], v128 offset:6144\n" \
  "s_waitcnt lgkmcnt(8)\n" \
  "v_mfma_f32_16x16x32_bf16 v[28:31], v[112:115], a[164:167], v[28:31]\n" \
  "ds_read_b128 v[80:83], v128 offset:1024\n" \
  "s_add_u32 s80, s80, 16384\n" \
  "v_mfma_f32_16x16x32_bf16 v[28:31], v[116:119], a[172:175], v[28:31]\n" \
  "v_mfma_f32_16x16x32_bf16 v[28:31], v[120:123], a[180:183], v[28:31]\n" \
  "ds_read_b128 v[84:87], v128 offset:3072\n" \
  "s_and_b32 s80, s80, 0xffff\n" \
  "v_mfma_f32_16x16x32_bf16 v[28:31], v[124:127], a[188:191], v[28:31]\n" \
  "v_mfma_f32_16x16x32_bf16 v[20:23], v[112:115], a[160:163], v[20:23]\n" \
  "ds_read_b128 v[88:91], v128 offset:5120\n" \
  "s_add_u32 m0, s68, 101376\n" \
  "v_mfma_f32_16x16x32_bf16 v[20:23], v[116:119], a[168:171], v[20:23]\n" \
  "buffer_load_dwordx4 v217, s[40:43], 0 offen sc1 lds\n" \
  "v_mfma_f32_16x16x32_bf16 v[20:23], v[120:123], a[176:179], v[20:23]\n" \
  "s_add_u32 m0, s68, 32768\n" \
  "v_mfma_f32_16x16x32_bf16 v[20:23], v[124:127], a[184:187], v[20:23]\n" \
  "ds_read_b128 v[92:95], v128 offset:7168\n" \
  "s_waitcnt lgkmcnt(4)\n" \
  "v_mfma_f32_16x16x32_bf16 v[0:3], v[64:67], a[192:195], v[0:3]\n" \
  "ds_read_b128 v[96:99], v128 offset:8192\n" \
  "buffer_load_dwordx4 v128, s[40:43], s82 offen offset:1024 sc1 lds\n" \
  "v_mfma_f32_16x16x32_bf16 v[0:3], v[68:71], a[200:203], v[0:3]\n" \
  "v_mfma_f32_16x16x32_bf16 v[0:3], v[72:75], a[208:211], v[0:3]\n" \
  "ds_read_b128 v[100:103], v128 offset:10240\n" \
  "buffer_load_dwordx4 v128, s[40:43], s82 offen offset:2048 sc1 lds\n" \
  "v_mfma_f32_16x16x32_bf16 v[0:3], v[76:79], a[216:219], v[0:3]\n" \
  "v_mfma_f32_16x16x32_bf16 v[8:11], v[64:67], a[196:199], v[8:11]\n" \
  "ds_read_b128 v[104:107], v128 offset:12288\n" \
  "buffer_load_dwordx4 v128, s[40:43], s82 offen offset:3072 sc1 lds\n" \
  "v_mfma_f32_16x16x32_bf16 v[8:11], v[68:71], a[204:207], v[8:11]\n" \
  "s_waitcnt vmcnt(13)\n" \
  "ds_read_b128 a[224:227], v140 offset:32768\n" \
  "v_mfma_f32_16x16x32_bf16 v[8:11], v[72:75], a[212:215], v[8:11]\n" \
  "ds_read_b128 a[228:231], v141 offset:32768\n" \
  "s_add_u32 s82, s81, s80\n" \
  "v_mfma_f32_16x16x32_bf16 v[8:11], v[76:79], a[220:223], v[8:11]\n" \
  "ds_read_b128 v[108:111], v128 offset:14336\n" \
  "s_waitcnt lgkmcnt(6)\n" \
  "v_mfma_f32_16x16x32_bf16 v[12:15], v[80:83], a[196:199], v[12:15]\n" \
  "ds_read_b128 v[112:115], v128 offset:9216\n" \
  "s_add_u32 m0, s68, 49152\n" \
  "v_mfma_f32_16x16x32_bf16 v[12:15], v[84:87], a[204:207], v[12:15]\n" \
  "ds_read_b128 a[232:235], v142 offset:32768\n" \
  "buffer_load_dwordx4 v128, s[40:43], s82 offen offset:0 sc1 lds\n" \
  "v_mfma_f32_16x16x32_bf16 v[12:15], v[88:91], a[212:215], v[12:15]\n" \
  "ds_read_b128 v[116:119], v128 offset:11264\n" \
  "s_add_u32 s80, s80, 16384\n" \
  "v_mfma_f32_16x16x32_bf16 v[12:15], v[92:95], a[220:223], v[12:15]\n" \
  "ds_read_b128 a[236:239], v143 offset:32768\n" \
  "s_and_b32 s80, s80, 0xffff\n" \
  "v_mfma_f32_16x16x32_bf16 v[4:7], v[80:83], a[192:195], v[4:7]\n" \
  "ds_read_b128 v[120:123], v128 offset:13312\n" \
  "buffer_load_dwordx4 v128, s[40:43], s82 offen offset:1024 sc1 lds\n" \
  "v_mfma_f32_16x16x32_bf16 v[4:7], v[84:87], a[200:203], v[4:7]\n" \
  "v_mfma_f32_16x16x32_bf16 v[4:7], v[88:91], a[208:211], v[4:7]\n" \
  "ds_read_b128 v[124:127], v128 offset:15360\n" \
  "buffer_load_dwordx4 v128, s[40:43], s82 offen offset:2048 sc1 lds\n" \
  "v_mfma_f32_16x16x32_bf16 v[4:7], v[92:95], a[216:219], v[4:7]\n" \
  "s_waitcnt vmcnt(15) lgkmcnt(6)\n" \
  "s_barrier\n" \
  "v_mfma_f32_16x16x32_bf16 v[16:19], v[96:99], a[192:195], v[16:19]\n" \
  "ds_read_b128 v[64:67], v128 offset:16384\n" \
  "buffer_load_dwordx4 v128, s[40:43], s82 offen offset:3072 sc1 lds\n" \
  "v_mfma_f32_16x16x32_bf16 v[16:19], v[100:103], a[200:203], v[16:19]\n" \
  "s_waitcnt vmcnt(12)\n" \
  "ds_read_b128 a[240:243], v140 offset:49152\n" \
  "v_mfma_f32_16x16x32_bf16 v[16:19], v[104:107], a[208:211], v[16:19]\n" \
  "ds_read_b128 v[68:71], v128 offset:18432\n" \
  "s_add_u32 s82, s81, s80\n" \
  "v_mfma_f32_16x16x32_bf16 v[16:19], v[108:111], a[216:219], v[16:19]\n" \
  "ds_read_b128 a[244:247], v141 offset:49152\n" \
  "s_add_u32 m0, s68, 65536\n" \
  "v_mfma_f32_16x16x32_bf16 v[24:27], v[96:99], a[196:199], v[24:27]\n" \
  "ds_read_b128 v[72:75], v128 offset:20480\n" \
  "buffer_load_dwordx4 v128, s[40:43], s82 offen offset:0 sc1 lds\n" \
  "v_mfma_f32_16x16x32_bf16 v[24:27], v[100:103], a[204:207], v[24:27]\n" \
  "ds_read_b128 a[248:251], v142 offset:49152\n" \
  "s_add_u32 s80, s80, 16384\n" \
  "v_mfma_f32_16x16x32_bf16 v[24:27], v[104:107], a[212:215], v[24:27]\n" \
  "ds_read_b128 a[252:255], v143 offset:49152\n" \
  "s_and_b32 s80, s80, 0xffff\n" \
  "v_mfma_f32_16x16x32_bf16 v[24:27], v[108:111], a[220:223], v[24:27]\n" \
  "ds_read_b128 v[76:79], v128 offset:22528\n" \
  "s_waitcnt lgkmcnt(8)\n" \
  "v_mfma_f32_16x16x32_bf16 v[28:31], v[112:115], a[196:199], v[28:31]\n" \
  "ds_read_b128 v[80:83], v128 offset:17408\n" \
  "v_mfma_f32_16x16x32_bf16 v[28:31], v[116:119], a[204:207], v[28:31]\n" \
  "v_mfma_f32_16x16x32_bf16 v[28:31], v[120:123], a[212:215], v[28:31]\n" \
  "ds_read_b128 v[84:87], v128 offset:19456\n" \
  "v_mfma_f32_16x16x32_bf16 v[28:31], v[124:127], a[220:223], v[28:31]\n" \
  "v_mfma_f32_16x16x32_bf16 v[20:23], v[112:115], a[192:195], v[20:23]\n" \
  "ds_read_b128 v[88:91], v128 offset:21504\n" \
  "buffer_load_dwordx4 v128, s[40:43], s82 offen offset:1024 sc1 lds\n" \
  "v_mfma_f32_16x16x32_bf16 v[20:23], v[116:119], a[200:203], v[20:23]\n" \
  "v_mfma_f32_16x16x32_bf16 v[20:23], v[120:123], a[208:211], v[20:23]\n" \
  "v_mfma_f32_16x16x32_bf16 v[20:23], v[124:127], a[216:219], v[20:23]\n" \
  "ds_read_b128 v[92:95], v128 offset:23552\n" \
  "s_waitcnt lgkmcnt(4)\n" \
  "v_mfma_f32_16x16x32_bf16 v[0:3], v[64:67], a[224:227], v[0:3]\n" \
  "ds_read_b128 v[96:99], v128 offset:24576\n" \
  "v_mfma_f32_16x16x32_bf16 v[0:3], v[68:71], a[232:235], v[0:3]\n" \
  "v_mfma_f32_16x16x32_bf16 v[0:3], v[72:75], a[240:243], v[0:3]\n" \
  "ds_read_b128 v[100:103], v128 offset:26624\n" \
  "v_mfma_f32_16x16x32_bf16 v[0:3], v[76:79], a[248:251], v[0:3]\n" \
  "v_mfma_f32_16x16x32_bf16 v[8:11], v[64:67], a[228:231], v[8:11]\n" \
  "ds_read_b128 v[104:107], v128 offset:28672\n" \
  "buffer_load_dwordx4 v128, s[40:43], s82 offen offset:2048 sc1 lds\n" \
  "v_mfma_f32_16x16x32_bf16 v[8:11], v[68:71], a[236:239], v[8:11]\n" \
  "v_mfma_f32_16x16x32_bf16 v[8:11], v[72:75], a[244:247], v[8:11]\n" \
  "v_mfma_f32_16x16x32_bf16 v[8:11], v[76:79], a[252:255], v[8:11]\n" \
  "ds_read_b128 v[108:111], v128 offset:30720\n" \
  "s_waitcnt lgkmcnt(4)\n" \
  "v_mfma_f32_16x16x32_bf16 v[12:15], v[80:83], a[228:231], v[12:15]\n" \
  "ds_read_b128 v[112:115], v128 offset:25600\n" \
  "v_mfma_f32_16x16x32_bf16 v[12:15], v[84:87], a[236:239], v[12:15]\n" \
  "v_mfma_f32_16x16x32_bf16 v[12:15], v[88:91], a[244:247], v[12:15]\n" \
  "ds_read_b128 v[116:119], v128 offset:27648\n" \
  "v_mfma_f32_16x16x32_bf16 v[12:15], v[92:95], a[252:255], v[12:15]\n" \
  "v_mfma_f32_16x16x32_bf16 v[4:7], v[80:83], a[224:227], v[4:7]\n" \
  "ds_read_b128 v[120:123], v128 offset:29696\n" \
  "buffer_load_dwordx4 v128, s[40:43], s82 offen offset:3072 sc1 lds\n" \
  "v_mfma_f32_16x16x32_bf16 v[4:7], v[84:87], a[232:235], v[4:7]\n" \
  "v_cvt_pk_bf16_f32 v228, v8, v9\n" \
  "v_cvt_pk_bf16_f32 v229, v10, v11\n" \
  "v_mfma_f32_16x16x32_bf16 v[4:7], v[88:91], a[240:243], v[4:7]\n" \
  "ds_read_b128 v[124:127], v128 offset:31744\n" \
  "v_mfma_f32_16x16x32_bf16 v[4:7], v[92:95], a[248:251], v[4:7]\n" \
  "s_waitcnt vmcnt(8) lgkmcnt(4)\n" \
  "s_barrier\n" \
  "v_mfma_f32_16x16x32_bf16 v[16:19], v[96:99], a[224:227], v[16:19]\n" \
  "ds_read_b128 v[64:67], v128 offset:32768\n" \
  "buffer_load_dword v248, v153, s[40:43], 0 offen\n" \
  "v_mfma_f32_16x16x32_bf16 v[16:19], v[100:103], a[232:235], v[16:19]\n" \
  "v_cvt_pk_bf16_f32 v230, v12, v13\n" \
  "v_cvt_pk_bf16_f32 v231, v14, v15\n" \
  "v_mfma_f32_16x16x32_bf16 v[16:19], v[104:107], a[240:243], v[16:19]\n" \
  "ds_read_b128 v[68:71], v128 offset:34816\n" \
  "s_add_u32 s82, s81, s80\n" \
  "v_mfma_f32_16x16x32_bf16 v[16:19], v[108:111], a[248:251], v[16:19]\n" \
  "buffer_store_dwordx4 v[228:231], v128, s[40:43], s88 offen offset:1024 sc1\n" \
  "v_mfma_f32_16x16x32_bf16 v[24:27], v[96:99], a[228:231], v[24:27]\n" \
  "ds_read_b128 v[72:75], v128 offset:36864\n" \
  "s_add_u32 m0, s68, 81920\n" \
  "v_mfma_f32_16x16x32_bf16 v[24:27], v[100:103], a[236:239], v[24:27]\n" \
  "buffer_load_dwordx4 v128, s[40:43], s82 offen offset:0 sc1 lds\n" \
  "s_add_u32 s80, s80, 16384\n" \
  "v_mfma_f32_16x16x32_bf16 v[24:27], v[104:107], a[244:247], v[24:27]\n" \
  "s_and_b32 s80, s80, 0xffff\n" \
  "s_add_u32 s81, s81, 0x40000\n" \
  "v_mfma_f32_16x16x32_bf16 v[24:27], v[108:111], a[252:255], v[24:27]\n" \
  "ds_read_b128 v[76:79], v128 offset:38912\n" \
  "s_waitcnt lgkmcnt(4)\n" \
  "v_mfma_f32_16x16x32_bf16 v[28:31], v[112:115], a[228:231], v[28:31]\n" \
  "ds_read_b128 v[80:83], v128 offset:33792\n" \
  "v_cvt_pk_bf16_f32 v224, v0, v1\n" \
  "v_mfma_f32_16x16x32_bf16 v[28:31], v[116:119], a[236:239], v[28:31]\n" \
  "v_cvt_pk_bf16_f32 v225, v2, v3\n" \
  "v_cvt_pk_bf16_f32 v226, v4, v5\n" \
  "v_mfma_f32_16x16x32_bf16 v[28:31], v[120:123], a[244:247], v[28:31]\n" \
  "ds_read_b128 v[84:87], v128 offset:35840\n" \
  "v_cvt_pk_bf16_f32 v227, v6, v7\n" \
  "v_mfma_f32_16x16x32_bf16 v[28:31], v[124:127], a[252:255], v[28:31]\n" \
  "buffer_store_dwordx4 v[224:227], v128, s[40:43], s88 offen offset:0 sc1\n" \
  "v_mfma_f32_16x16x32_bf16 v[20:23], v[112:115], a[224:227], v[20:23]\n" \
  "ds_read_b128 v[88:91], v128 offset:37888\n" \
  "buffer_load_dwordx4 v128, s[40:43], s82 offen offset:1024 sc1 lds\n" \
  "v_mfma_f32_16x16x32_bf16 v[20:23], v[116:119], a[232:235], v[20:23]\n" \
  "buffer_load_dwordx4 v[204:207], v152, s[40:43], 0 offen sc1\n" \
  "s_nop 0\n" \
  "v_mfma_f32_16x16x32_bf16 v[20:23], v[120:123], a[240:243], v[20:23]\n" \
  "PAD4_%=:\n" \
  "s_cbranch_execz PAD8_%=\n" \
  "ds_read_b64 v[242:243], v58 offset:3072\n" \
  "v_mfma_f32_16x16x32_bf16 v[20:23], v[124:127], a[248:251], v[20:23]\n" \
  "ds_read_b128 v[92:95], v128 offset:39936\n" \
  "s_waitcnt lgkmcnt(5)\n" \
  "v_mfma_f32_16x16x32_bf16 v[0:3], v[64:67], a[0:3], 0\n" \
  "ds_read_b128 v[96:99], v128 offset:40960\n" \
  "v_mfma_f32_16x16x32_bf16 v[0:3], v[68:71], a[8:11], v[0:3]\n" \
  "ds_read_b64 v[244:245], v59 offset:3072\n" \
  "v_mfma_f32_16x16x32_bf16 v[0:3], v[72:75], a[16:19], v[0:3]\n" \
  "ds_read_b128 v[100:103], v128 offset:43008\n" \
  "v_mfma_f32_16x16x32_bf16 v[0:3], v[76:79], a[24:27], v[0:3]\n" \
  "ds_read_b128 v[200:203], v218 offset:3328\n" \
  "v_mfma_f32_16x16x32_bf16 v[8:11], v[64:67], a[4:7], 0\n" \
  "ds_read_b128 v[104:107], v128 offset:45056\n" \
  "buffer_load_dwordx4 v128, s[40:43], s82 offen offset:2048 sc1 lds\n" \
  "v_mfma_f32_16x16x32_bf16 v[8:11], v[68:71], a[12:15], v[8:11]\n" \
  "ds_read_b128 v[232:235], v215\n" \
  "v_mfma_f32_16x16x32_bf16 v[8:11], v[72:75], a[20:23], v[8:11]\n" \
  "ds_read_b128 v[236:239], v215 offset:1024\n" \
  "v_mfma_f32_16x16x32_bf16 v[8:11], v[76:79], a[28:31], v[8:11]\n" \
  "ds_read_b128 v[108:111], v128 offset:47104\n" \
  "s_waitcnt lgkmcnt(8)\n" \
  "v_mfma_f32_16x16x32_bf16 v[12:15], v[80:83], a[4:7], 0\n" \
  "ds_read_b128 v[112:115], v128 offset:41984\n" \
  "v_mfma_f32_16x16x32_bf16 v[12:15], v[84:87], a[12:15], v[12:15]\n" \
  "ds_read_b64 v[240:241], v216 offset:2048\n" \
  "v_mfma_f32_16x16x32_bf16 v[12:15], v[88:91], a[20:23], v[12:15]\n" \
  "ds_read_b128 v[116:119], v128 offset:44032\n" \
  "v_mfma_f32_16x16x32_bf16 v[12:15], v[92:95], a[28:31], v[12:15]\n" \
  "s_waitcnt lgkmcnt(7)\n" \
  "v_xor_b32 v48, s71, v201\n" \
  "v_mfma_f32_16x16x32_bf16 v[4:7], v[80:83], a[0:3], 0\n" \
  "ds_read_b128 v[120:123], v128 offset:46080\n" \
  "buffer_load_dwordx4 v128, s[40:43], s82 offen offset:3072 sc1 lds\n" \
  "v_mfma_f32_16x16x32_bf16 v[4:7], v[84:87], a[8:11], v[4:7]\n" \
  "v_bitop3_b32 v48, v203, v48, s71 bitop3:0xde\n" \
  "v_cndmask_b32 v48, 0, v48, s[72:73]\n" \
  "v_mfma_f32_16x16x32_bf16 v[4:7], v[88:91], a[16:19], v[4:7]\n" \
  "ds_read_b128 v[124:127], v128 offset:48128\n" \
  "v_bitop3_b32 v48, v243, v48, s71 bitop3:0xde\n" \
  "v_mfma_f32_16x16x32_bf16 v[4:7], v[92:95], a[24:27], v[4:7]\n" \
  "s_waitcnt vmcnt(12) lgkmcnt(5)\n" \
  "s_barrier\n" \
  "v_mfma_f32_16x16x32_bf16 v[0:3], v[96:99], a[32:35], v[0:3]\n" \
  "ds_read_b128 v[64:67], v128 offset:49152\n" \
  "v_bitop3_b32 v48, v245, v48, s71 bitop3:0xde\n" \
  "v_mfma_f32_16x16x32_bf16 v[0:3], v[100:103], a[40:43], v[0:3]\n" \
  "v_cmp_ne_u32 vcc, 0, v48\n" \
  "s_cbranch_vccnz STS_%=\n" \
  "STK_%=:\n" \
  "v_mfma_f32_16x16x32_bf16 v[0:3], v[104:107], a[48:51], v[0:3]\n" \
  "ds_read_b128 v[68:71], v128 offset:51200\n" \
  "s_add_u32 s82, s81, s80\n" \
  "v_mfma_f32_16x16x32_bf16 v[0:3], v[108:111], a[56:59], v[0:3]\n" \
  "v_readfirstlane_b32 s72, v200\n" \
  "v_mfma_f32_16x16x32_bf16 v[8:11], v[96:99], a[36:39], v[8:11]\n" \
  "ds_read_b128 v[72:75], v128 offset:53248\n" \
  "s_add_u32 m0, s68, 0\n" \
  "v_mfma_f32_16x16x32_bf16 v[8:11], v[100:103], a[44:47], v[8:11]\n" \
  "buffer_load_dwordx4 v128, s[40:43], s82 offen offset:0 sc1 lds\n" \
  "s_add_u32 s80, s80, 16384\n" \
  "v_mfma_f32_16x16x32_bf16 v[8:11], v[104:107], a[52:55], v[8:11]\n" \
  "s_and_b32 s80, s80, 0xffff\n" \
  "v_cvt_pk_bf16_f32 v228, v24, v25\n" \
  "v_mfma_f32_16x16x32_bf16 v[8:11], v[108:111], a[60:63], v[8:11]\n" \
  "ds_read_b128 v[76:79], v128 offset:55296\n" \
  "s_waitcnt lgkmcnt(4)\n" \
  "v_mfma_f32_16x16x32_bf16 v[12:15], v[112:115], a[36:39], v[12:15]\n" \
  "ds_read_b128 v[80:83], v128 offset:50176\n" \
  "v_cvt_pk_bf16_f32 v229, v26, v27\n" \
  "v_mfma_f32_16x16x32_bf16 v[12:15], v[116:119], a[44:47], v[12:15]\n" \
  "v_cvt_pk_bf16_f32 v230, v28, v29\n" \
  "v_cvt_pk_bf16_f32 v231, v30, v31\n" \
  "v_mfma_f32_16x16x32_bf16 v[12:15], v[120:123], a[52:55], v[12:15]\n" \
  "ds_read_b128 v[84:87], v128 offset:52224\n" \
  "v_mfma_f32_16x16x32_bf16 v[12:15], v[124:127], a[60:63], v[12:15]\n" \
  "buffer_store_dwordx4 v[228:231], v128, s[40:43], s89 offen offset:0 sc1\n" \
  "v_mfma_f32_16x16x32_bf16 v[4:7], v[112:115], a[32:35], v[4:7]\n" \
  "ds_read_b128 v[88:91], v128 offset:54272\n" \
  "buffer_load_dwordx4 v128, s[40:43], s82 offen offset:1024 sc1 lds\n" \
  "v_mfma_f32_16x16x32_bf16 v[4:7], v[116:119], a[40:43], v[4:7]\n" \
  "v_cvt_pk_bf16_f32 v224, v16, v17\n" \
  "v_cvt_pk_bf16_f32 v225, v18, v19\n" \
  "v_mfma_f32_16x16x32_bf16 v[4:7], v[120:123], a[48:51], v[4:7]\n" \
  "v_cvt_pk_bf16_f32 v226, v20, v21\n" \
  "v_cvt_pk_bf16_f32 v227, v22, v23\n" \
  "v_mfma_f32_16x16x32_bf16 v[4:7], v[124:127], a[56:59], v[4:7]\n" \
  "ds_read_b128 v[92:95], v128 offset:56320\n" \
  "s_waitcnt lgkmcnt(4)\n" \
  "v_mfma_f32_16x16x32_bf16 v[0:3], v[64:67], a[64:67], v[0:3]\n" \
  "ds_read_b128 v[96:99], v128 offset:57344\n" \
  "v_mfma_f32_16x16x32_bf16 v[0:3], v[68:71], a[72:75], v[0:3]\n" \
  "buffer_store_dwordx4 v[224:227], v128, s[40:43], s89 offen offset:1024 sc1\n" \
  "v_mfma_f32_16x16x32_bf16 v[0:3], v[72:75], a[80:83], v[0:3]\n" \
  "ds_read_b128 v[100:103], v128 offset:59392\n" \
  "v_mfma_f32_16x16x32_bf16 v[0:3], v[76:79], a[88:91], v[0:3]\n" \
  "v_readfirstlane_b32 s78, v202\n" \
  "v_mfma_f32_16x16x32_bf16 v[8:11], v[64:67], a[68:71], v[8:11]\n" \
  "ds_read_b128 v[104:107], v128 offset:61440\n" \
  "buffer_load_dwordx4 v128, s[40:43], s82 offen offset:2048 sc1 lds\n" \
  "v_mfma_f32_16x16x32_bf16 v[8:11], v[68:71], a[76:79], v[8:11]\n" \
  "s_movk_i32 s62, 2048\n" \
  "s_lshr_b32 s73, s72, 16\n" \
  "v_mfma_f32_16x16x32_bf16 v[8:11], v[72:75], a[84:87], v[8:11]\n" \
  "s_and_b32 s72, s72, 0xffff\n" \
  "v_lshl_add_u32 v149, v149, 1, s62\n" \
  "v_mfma_f32_16x16x32_bf16 v[8:11], v[76:79], a[92:95], v[8:11]\n" \
  "ds_read_b128 v[108:111], v128 offset:63488\n" \
  "s_waitcnt lgkmcnt(4)\n" \
  "v_mfma_f32_16x16x32_bf16 v[12:15], v[80:83], a[68:71], v[12:15]\n" \
  "ds_read_b128 v[112:115], v128 offset:58368\n" \
  "v_lshl_add_u32 v151, v151, 1, s62\n" \
  "v_mfma_f32_16x16x32_bf16 v[12:15], v[84:87], a[76:79], v[12:15]\n" \
  "v_mov_b32 v55, 2560\n" \
  "v_mov_b32 v56, 33554432\n" \
  "v_mfma_f32_16x16x32_bf16 v[12:15], v[88:91], a[84:87], v[12:15]\n" \
  "ds_read_b128 v[116:119], v128 offset:60416\n" \
  "v_cmp_eq_u32 s[94:95], s73, v148\n" \
  "v_mfma_f32_16x16x32_bf16 v[12:15], v[92:95], a[92:95], v[12:15]\n" \
  "v_cmp_eq_u32 s[60:61], s72, v148\n" \
  "v_mfma_f32_16x16x32_bf16 v[4:7], v[80:83], a[64:67], v[4:7]\n" \
  "ds_read_b128 v[120:123], v128 offset:62464\n" \
  "buffer_load_dwordx4 v128, s[40:43], s82 offen offset:3072 sc1 lds\n" \
  "v_mfma_f32_16x16x32_bf16 v[4:7], v[84:87], a[72:75], v[4:7]\n" \
  "v_cndmask_b32 v57, v56, v55, s[94:95]\n" \
  "v_cndmask_b32 v57, v57, 0, s[60:61]\n" \
  "v_mfma_f32_16x16x32_bf16 v[4:7], v[88:91], a[80:83], v[4:7]\n" \
  "ds_read_b128 v[124:127], v128 offset:64512\n" \
  "s_or_b64 s[94:95], s[94:95], s[60:61]\n" \
  "v_mfma_f32_16x16x32_bf16 v[4:7], v[92:95], a[88:91], v[4:7]\n" \
  "s_waitcnt vmcnt(14) lgkmcnt(4)\n" \
  "s_barrier\n" \
  "v_mfma_f32_16x16x32_bf16 v[0:3], v[96:99], a[96:99], v[0:3]\n" \
  "ds_read_b128 v[64:67], v129 offset:0\n" \
  "v_add_u32 v150, v149, v57\n" \
  "v_mfma_f32_16x16x32_bf16 v[0:3], v[100:103], a[104:107], v[0:3]\n" \
  "v_cmp_eq_u32 s[92:93], s73, v147\n" \
  "v_cmp_eq_u32 s[60:61], s72, v147\n" \
  "v_mfma_f32_16x16x32_bf16 v[0:3], v[104:107], a[112:115], v[0:3]\n" \
  "ds_read_b128 v[68:71], v129 offset:2048\n" \
  "s_add_u32 s82, s81, s80\n" \
  "v_mfma_f32_16x16x32_bf16 v[0:3], v[108:111], a[120:123], v[0:3]\n" \
  "v_cndmask_b32 v57, v56, v55, s[92:93]\n" \
  "v_cndmask_b32 v57, v57, 0, s[60:61]\n" \
  "v_mfma_f32_16x16x32_bf16 v[8:11], v[96:99], a[100:103], v[8:11]\n" \
  "ds_read_b128 v[72:75], v129 offset:4096\n" \
  "s_add_u32 m0, s68, 16384\n" \
  "v_mfma_f32_16x16x32_bf16 v[8:11], v[100:103], a[108:111], v[8:11]\n" \
  "buffer_load_dwordx4 v128, s[40:43], s82 offen offset:0 sc1 lds\n" \
  "s_add_u32 s80, s80, 16384\n" \
  "v_mfma_f32_16x16x32_bf16 v[8:11], v[104:107], a[116:119], v[8:11]\n" \
  "s_and_b32 s80, s80, 0xffff\n" \
  "s_or_b64 s[92:93], s[92:93], s[60:61]\n" \
  "v_mfma_f32_16x16x32_bf16 v[8:11], v[108:111], a[124:127], v[8:11]\n" \
  "ds_read_b128 v[76:79], v129 offset:6144\n" \
  "s_waitcnt lgkmcnt(4)\n" \
  "v_mfma_f32_16x16x32_bf16 v[12:15], v[112:115], a[100:103], v[12:15]\n" \
  "ds_read_b128 v[80:83], v129 offset:1024\n" \
  "s_waitcnt vmcnt(11)\n" \
  "v_mfma_f32_16x16x32_bf16 v[12:15], v[116:119], a[108:111], v[12:15]\n" \
  "v_add_u32 v149, v149, v57\n" \
  "v_add_u32 v151, v151, v57\n" \
  "v_mfma_f32_16x16x32_bf16 v[12:15], v[120:123], a[116:119], v[12:15]\n" \
  "ds_read_b128 v[84:87], v129 offset:3072\n" \
  "buffer_store_dword v135, v50, s[40:43], s88 offen offset:3072 sc1\n" \
  "v_mfma_f32_16x16x32_bf16 v[12:15], v[124:127], a[124:127], v[12:15]\n" \
  "s_waitcnt vmcnt(10)\n" \
  "v_xor_b32 v208, s71, v205\n" \
  "v_mfma_f32_16x16x32_bf16 v[4:7], v[112:115], a[96:99], v[4:7]\n" \
  "ds_read_b128 v[88:91], v129 offset:5120\n" \
  "buffer_load_dwordx4 v128, s[40:43], s82 offen offset:1024 sc1 lds\n" \
  "v_mfma_f32_16x16x32_bf16 v[4:7], v[116:119], a[104:107], v[4:7]\n" \
  "v_bitop3_b32 v208, v207, v208, s71 bitop3:0xde\n" \
  "v_and_b32 v208, v208, v154\n" \
  "v_mfma_f32_16x16x32_bf16 v[4:7], v[120:123], a[112:115], v[4:7]\n" \
  "v_cmp_ne_u32 vcc, 0, v208\n" \
  "s_cbranch_vccnz RCS_%=\n" \
  "RCK_%=:\n" \
  "v_mfma_f32_16x16x32_bf16 v[4:7], v[124:127], a[120:123], v[4:7]\n" \
  "ds_read_b128 v[92:95], v129 offset:7168\n" \
  "s_waitcnt lgkmcnt(4)\n" \
  "v_mfma_f32_16x16x32_bf16 v[0:3], v[64:67], a[128:131], v[0:3]\n" \
  "ds_read_b128 v[96:99], v129 offset:8192\n" \
  "s_nop 0\n" \
  "v_mfma_f32_16x16x32_bf16 v[0:3], v[68:71], a[136:139], v[0:3]\n" \
  "v_readlane_b32 s60, v204, 27\n" \
  "v_mfma_f32_16x16x32_bf16 v[0:3], v[72:75], a[144:147], v[0:3]\n" \
  "ds_read_b128 v[100:103], v129 offset:10240\n" \
  "s_mov_b32 s91, 0x3fb8aa3b\n" \
  "v_mfma_f32_16x16x32_bf16 v[0:3], v[76:79], a[152:155], v[0:3]\n" \
  "v_readlane_b32 s61, v206, 27\n" \
  "v_mfma_f32_16x16x32_bf16 v[8:11], v[64:67], a[132:135], v[8:11]\n" \
  "ds_read_b128 v[104:107], v129 offset:12288\n" \
  "buffer_load_dwordx4 v128, s[40:43], s82 offen offset:2048 sc1 lds\n" \
  "v_mfma_f32_16x16x32_bf16 v[8:11], v[68:71], a[140:143], v[8:11]\n" \
  "v_writelane_b32 v204, 0, 27\n" \
  "v_writelane_b32 v206, 0, 27\n" \
  "v_mfma_f32_16x16x32_bf16 v[8:11], v[72:75], a[148:151], v[8:11]\n" \
  "v_and_b32 v60, 0xffff0000, v242\n" \
  "v_mfma_f32_16x16x32_bf16 v[8:11], v[76:79], a[156:159], v[8:11]\n" \
  "ds_read_b128 v[108:111], v129 offset:14336\n" \
  "s_waitcnt lgkmcnt(4)\n" \
  "v_mfma_f32_16x16x32_bf16 v[12:15], v[80:83], a[132:135], v[12:15]\n" \
  "ds_read_b128 v[112:115], v129 offset:9216\n" \
  "v_mov_b32 v210, s60\n" \
  "v_mfma_f32_16x16x32_bf16 v[12:15], v[84:87], a[140:143], v[12:15]\n" \
  "v_rcp_f32 v60, v60\n" \
  "v_mfma_f32_16x16x32_bf16 v[12:15], v[88:91], a[148:151], v[12:15]\n" \
  "ds_read_b128 v[116:119], v129 offset:11264\n" \
  "v_mov_b32 v209, s61\n" \
  "v_mfma_f32_16x16x32_bf16 v[12:15], v[92:95], a[156:159], v[12:15]\n" \
  "v_lshlrev_b32 v62, 16, v242\n" \
  "v_mul_f32 v60, s91, v60\n" \
  "v_mfma_f32_16x16x32_bf16 v[4:7], v[80:83], a[128:131], v[4:7]\n" \
  "ds_read_b128 v[120:123], v129 offset:13312\n" \
  "buffer_load_dwordx4 v128, s[40:43], s82 offen offset:3072 sc1 lds\n" \
  "v_mfma_f32_16x16x32_bf16 v[4:7], v[84:87], a[136:139], v[4:7]\n" \
  "v_mul_f32 v62, v62, v60\n" \
  "v_cndmask_b32 v60, v60, 0, s[92:93]\n" \
  "v_mfma_f32_16x16x32_bf16 v[4:7], v[88:91], a[144:147], v[4:7]\n" \
  "ds_read_b128 v[124:127], v129 offset:15360\n" \
  "v_permlane16_swap_b32 v204, v206\n" \
  "v_mfma_f32_16x16x32_bf16 v[4:7], v[92:95], a[152:155], v[4:7]\n" \
  "s_waitcnt vmcnt(11) lgkmcnt(4)\n" \
  "s_barrier\n" \
  "v_mfma_f32_16x16x32_bf16 v[0:3], v[96:99], a[160:163], v[0:3]\n" \
  "ds_read_b128 v[64:67], v129 offset:16384\n" \
  "v_permlane16_swap_b32 v210, v209\n" \
  "v_mfma_f32_16x16x32_bf16 v[0:3], v[100:103], a[168:171], v[0:3]\n" \
  "v_add_f32 v208, v204, v206\n" \
  "v_mov_b32 v209, v208\n" \
  "v_mfma_f32_16x16x32_bf16 v[0:3], v[104:107], a[176:179], v[0:3]\n" \
  "ds_read_b128 v[68:71], v129 offset:18432\n" \
  "s_add_u32 s82, s81, s80\n" \
  "v_mfma_f32_16x16x32_bf16 v[0:3], v[108:111], a[184:187], v[0:3]\n" \
  "v_permlane32_swap_b32 v208, v209\n" \
  "v_cndmask_b32 v62, -v62, 0, s[92:93]\n" \
  "v_mfma_f32_16x16x32_bf16 v[8:11], v[96:99], a[164:167], v[8:11]\n" \
  "ds_read_b128 v[72:75], v129 offset:20480\n" \
  "s_add_u32 m0, s68, 32768\n" \
  "v_mfma_f32_16x16x32_bf16 v[8:11], v[100:103], a[172:175], v[8:11]\n" \
  "buffer_load_dwordx4 v128, s[40:43], s82 offen offset:0 sc1 lds\n" \
  "s_add_u32 s80, s80, 16384\n" \
  "v_mfma_f32_16x16x32_bf16 v[8:11], v[104:107], a[180:183], v[8:11]\n" \
  "s_and_b32 s80, s80, 0xffff\n" \
  "v_add_f32 v208, v208, v209\n" \
  "v_mfma_f32_16x16x32_bf16 v[8:11], v[108:111], a[188:191], v[8:11]\n" \
  "ds_read_b128 v[76:79], v129 offset:22528\n" \
  "s_waitcnt lgkmcnt(4)\n" \
  "v_mfma_f32_16x16x32_bf16 v[12:15], v[112:115], a[164:167], v[12:15]\n" \
  "ds_read_b128 v[80:83], v129 offset:17408\n" \
  "v_add_f32_dpp v208, v208, v208 quad_perm:[1,0,3,2] row_mask:0xf bank_mask:0xf bound_ctrl:1\n" \
  "v_mfma_f32_16x16x32_bf16 v[12:15], v[116:119], a[172:175], v[12:15]\n" \
  "v_and_b32 v61, 0xffff0000, v244\n" \
  "v_mfma_f32_16x16x32_bf16 v[12:15], v[120:123], a[180:183], v[12:15]\n" \
  "ds_read_b128 v[84:87], v129 offset:19456\n" \
  "v_add_f32_dpp v208, v208, v208 quad_perm:[2,3,0,1] row_mask:0xf bank_mask:0xf bound_ctrl:1\n" \
  "v_mfma_f32_16x16x32_bf16 v[12:15], v[124:127], a[188:191], v[12:15]\n" \
  "v_rcp_f32 v61, v61\n" \
  "v_mfma_f32_16x16x32_bf16 v[4:7], v[112:115], a[160:163], v[4:7]\n" \
  "ds_read_b128 v[88:91], v129 offset:21504\n" \
  "buffer_load_dwordx4 v128, s[40:43], s82 offen offset:1024 sc1 lds\n" \
  "v_mfma_f32_16x16x32_bf16 v[4:7], v[116:119], a[168:171], v[4:7]\n" \
  "s_waitcnt vmcnt(9)\n" \
  "buffer_store_dword v135, v50, s[40:43], s89 offen offset:3072 sc1\n" \
  "v_mfma_f32_16x16x32_bf16 v[4:7], v[120:123], a[176:179], v[4:7]\n" \
  "v_add_f32_dpp v208, v208, v208 row_half_mirror row_mask:0xf bank_mask:0xf bound_ctrl:1\n" \
  "v_lshlrev_b32 v63, 16, v244\n" \
  "v_mfma_f32_16x16x32_bf16 v[4:7], v[124:127], a[184:187], v[4:7]\n" \
  "ds_read_b128 v[92:95], v129 offset:23552\n" \
  "s_waitcnt lgkmcnt(4)\n" \
  "v_mfma_f32_16x16x32_bf16 v[0:3], v[64:67], a[192:195], v[0:3]\n" \
  "ds_read_b128 v[96:99], v129 offset:24576\n" \
  "v_add_f32_dpp v208, v208, v208 row_mirror row_mask:0xf bank_mask:0xf bound_ctrl:1\n" \
  "v_mfma_f32_16x16x32_bf16 v[0:3], v[68:71], a[200:203], v[0:3]\n" \
  "v_mul_f32 v61, s91, v61\n" \
  "v_mul_f32 v63, v63, v61\n" \
  "v_mfma_f32_16x16x32_bf16 v[0:3], v[72:75], a[208:211], v[0:3]\n" \
  "ds_read_b128 v[100:103], v129 offset:26624\n" \
  "s_lshr_b32 s60, s76, 1\n" \
  "v_mfma_f32_16x16x32_bf16 v[0:3], v[76:79], a[216:219], v[0:3]\n" \
  "s_lshl_b32 s60, s60, 4\n" \
  "s_lshr_b32 s60, s78, s60\n" \
  "v_mfma_f32_16x16x32_bf16 v[8:11], v[64:67], a[196:199], v[8:11]\n" \
  "ds_read_b128 v[104:107], v129 offset:28672\n" \
  "buffer_load_dwordx4 v128, s[40:43], s82 offen offset:2048 sc1 lds\n" \
  "v_mfma_f32_16x16x32_bf16 v[8:11], v[68:71], a[204:207], v[8:11]\n" \
  "s_lshl_b32 s60, s60, 16\n" \
  "v_mov_b32 v211, 0xbfb8aa3b\n" \
  "v_mfma_f32_16x16x32_bf16 v[8:11], v[72:75], a[212:215], v[8:11]\n" \
  "v_mul_f32 v211, s60, v211\n" \
  "v_cndmask_b32 v61, v61, 0, s[94:95]\n" \
  "v_mfma_f32_16x16x32_bf16 v[8:11], v[76:79], a[220:223], v[8:11]\n" \
  "ds_read_b128 v[108:111], v129 offset:30720\n" \
  "s_waitcnt lgkmcnt(4)\n" \
  "v_mfma_f32_16x16x32_bf16 v[12:15], v[80:83], a[196:199], v[12:15]\n" \
  "ds_read_b128 v[112:115], v129 offset:25600\n" \
  "v_cndmask_b32 v63, -v63, 0, s[94:95]\n" \
  "v_mfma_f32_16x16x32_bf16 v[12:15], v[84:87], a[204:207], v[12:15]\n" \
  "v_exp_f32 v211, v211\n" \
  "v_mfma_f32_16x16x32_bf16 v[12:15], v[88:91], a[212:215], v[12:15]\n" \
  "ds_read_b128 v[116:119], v129 offset:27648\n" \
  "v_add_f32 v211, 1.0, v211\n" \
  "v_mfma_f32_16x16x32_bf16 v[12:15], v[92:95], a[220:223], v[12:15]\n" \
  "v_rcp_f32 v211, v211\n" \
  "v_mfma_f32_16x16x32_bf16 v[4:7], v[80:83], a[192:195], v[4:7]\n" \
  "ds_read_b128 v[120:123], v129 offset:29696\n" \
  "buffer_load_dwordx4 v128, s[40:43], s82 offen offset:3072 sc1 lds\n" \
  "v_mfma_f32_16x16x32_bf16 v[4:7], v[84:87], a[200:203], v[4:7]\n" \
  "v_lshlrev_b32 v160, 16, v232\n" \
  "v_and_b32 v161, 0xffff0000, v232\n" \
  "v_mfma_f32_16x16x32_bf16 v[4:7], v[88:91], a[208:211], v[4:7]\n" \
  "ds_read_b128 v[124:127], v129 offset:31744\n" \
  "v_cvt_pk_bf16_f32 v211, v211, v211\n" \
  "v_mfma_f32_16x16x32_bf16 v[4:7], v[92:95], a[216:219], v[4:7]\n" \
  "s_waitcnt vmcnt(10) lgkmcnt(4)\n" \
  "s_barrier\n" \
  "v_mfma_f32_16x16x32_bf16 v[0:3], v[96:99], a[224:227], v[0:3]\n" \
  "ds_read_b128 v[64:67], v130 offset:0\n" \
  "v_lshlrev_b32 v211, 16, v211\n" \
  "v_mfma_f32_16x16x32_bf16 v[0:3], v[100:103], a[232:235], v[0:3]\n" \
  "v_cvt_pk_bf16_f32 v208, v208, v208\n" \
  "v_lshlrev_b32 v208, 16, v208\n" \
  "v_mfma_f32_16x16x32_bf16 v[0:3], v[104:107], a[240:243], v[0:3]\n" \
  "ds_read_b128 v[68:71], v130 offset:2048\n" \
  "s_add_u32 s82, s81, s80\n" \
  "v_mfma_f32_16x16x32_bf16 v[0:3], v[108:111], a[248:251], v[0:3]\n" \
  "v_mul_f32 v208, v208, v211\n" \
  "v_cvt_pk_bf16_f32 v208, v208, v208\n" \
  "v_mfma_f32_16x16x32_bf16 v[8:11], v[96:99], a[228:231], v[8:11]\n" \
  "ds_read_b128 v[72:75], v130 offset:4096\n" \
  "s_add_u32 m0, s68, 49152\n" \
  "v_mfma_f32_16x16x32_bf16 v[8:11], v[100:103], a[236:239], v[8:11]\n" \
  "buffer_load_dwordx4 v128, s[40:43], s82 offen offset:0 sc1 lds\n" \
  "s_add_u32 s80, s80, 16384\n" \
  "v_mfma_f32_16x16x32_bf16 v[8:11], v[104:107], a[244:247], v[8:11]\n" \
  "s_and_b32 s80, s80, 0xffff\n" \
  "s_add_u32 s81, s81, 0x40000\n" \
  "v_mfma_f32_16x16x32_bf16 v[8:11], v[108:111], a[252:255], v[8:11]\n" \
  "ds_read_b128 v[76:79], v130 offset:6144\n" \
  "s_waitcnt lgkmcnt(4)\n" \
  "v_mfma_f32_16x16x32_bf16 v[12:15], v[112:115], a[228:231], v[12:15]\n" \
  "ds_read_b128 v[80:83], v131 offset:0\n" \
  "v_lshlrev_b32 v208, 16, v208\n" \
  "v_mfma_f32_16x16x32_bf16 v[12:15], v[116:119], a[236:239], v[12:15]\n" \
  "v_lshlrev_b32 v162, 16, v233\n" \
  "v_fma_f32 v180, v160, v60, v62\n" \
  "v_mfma_f32_16x16x32_bf16 v[12:15], v[120:123], a[244:247], v[12:15]\n" \
  "ds_read_b128 v[84:87], v131 offset:2048\n" \
  "v_add_f32 v208, v208, v210\n" \
  "v_mfma_f32_16x16x32_bf16 v[12:15], v[124:127], a[252:255], v[12:15]\n" \
  "v_cvt_pk_bf16_f32 v208, v208, v208\n" \
  "v_lshlrev_b32 v208, 16, v208\n" \
  "v_mfma_f32_16x16x32_bf16 v[4:7], v[112:115], a[224:227], v[4:7]\n" \
  "ds_read_b128 v[88:91], v131 offset:4096\n" \
  "buffer_load_dwordx4 v128, s[40:43], s82 offen offset:1024 sc1 lds\n" \
  "v_mfma_f32_16x16x32_bf16 v[4:7], v[116:119], a[232:235], v[4:7]\n" \
  "v_cvt_pk_bf16_f32 v228, v8, v9\n" \
  "v_cvt_pk_bf16_f32 v229, v10, v11\n" \
  "v_mfma_f32_16x16x32_bf16 v[4:7], v[120:123], a[240:243], v[4:7]\n" \
  "v_mul_f32 v209, 0xbfb8aa3b, v208\n" \
  "v_fma_f32 v181, v161, v60, v62\n" \
  "v_mfma_f32_16x16x32_bf16 v[4:7], v[124:127], a[248:251], v[4:7]\n" \
  "ds_read_b128 v[92:95], v131 offset:6144\n" \
  "s_waitcnt lgkmcnt(4)\n" \
  "v_mfma_f32_16x16x32_bf16 v[16:19], v[64:67], a[0:3], 0\n" \
  "ds_read_b128 v[96:99], v130 offset:8192\n" \
  "v_fma_f32 v182, v162, v60, v62\n" \
  "v_mfma_f32_16x16x32_bf16 v[16:19], v[68:71], a[8:11], v[16:19]\n" \
  "v_cvt_pk_bf16_f32 v230, v12, v13\n" \
  "v_cvt_pk_bf16_f32 v231, v14, v15\n" \
  "v_mfma_f32_16x16x32_bf16 v[16:19], v[72:75], a[16:19], v[16:19]\n" \
  "ds_read_b128 v[100:103], v130 offset:10240\n" \
  "v_mfma_f32_16x16x32_bf16 v[16:19], v[76:79], a[24:27], v[16:19]\n" \
  "buffer_store_dwordx4 v[228:231], v128, s[40:43], s90 offen offset:0 sc1\n" \
  "v_mfma_f32_16x16x32_bf16 v[24:27], v[64:67], a[4:7], 0\n" \
  "ds_read_b128 v[104:107], v130 offset:12288\n" \
  "buffer_load_dwordx4 v128, s[40:43], s82 offen offset:2048 sc1 lds\n" \
  "v_mfma_f32_16x16x32_bf16 v[24:27], v[68:71], a[12:15], v[24:27]\n" \
  "v_cvt_pk_bf16_f32 v224, v0, v1\n" \
  "v_cvt_pk_bf16_f32 v225, v2, v3\n" \
  "v_mfma_f32_16x16x32_bf16 v[24:27], v[72:75], a[20:23], v[24:27]\n" \
  "v_cvt_pk_bf16_f32 v226, v4, v5\n" \
  "v_cvt_pk_bf16_f32 v227, v6, v7\n" \
  "v_mfma_f32_16x16x32_bf16 v[24:27], v[76:79], a[28:31], v[24:27]\n" \
  "ds_read_b128 v[108:111], v130 offset:14336\n" \
  "s_waitcnt lgkmcnt(4)\n" \
  "v_mfma_f32_16x16x32_bf16 v[28:31], v[80:83], a[4:7], 0\n" \
  "ds_read_b128 v[112:115], v131 offset:8192\n" \
  "v_mfma_f32_16x16x32_bf16 v[28:31], v[84:87], a[12:15], v[28:31]\n" \
  "buffer_store_dwordx4 v[224:227], v128, s[40:43], s90 offen offset:1024 sc1\n" \
  "v_mfma_f32_16x16x32_bf16 v[28:31], v[88:91], a[20:23], v[28:31]\n" \
  "ds_read_b128 v[116:119], v131 offset:10240\n" \
  "v_mfma_f32_16x16x32_bf16 v[28:31], v[92:95], a[28:31], v[28:31]\n" \
  "v_exp_f32 v209, v209\n" \
  "v_mfma_f32_16x16x32_bf16 v[20:23], v[80:83], a[0:3], 0\n" \
  "ds_read_b128 v[120:123], v131 offset:12288\n" \
  "buffer_load_dwordx4 v128, s[40:43], s82 offen offset:3072 sc1 lds\n" \
  "v_mfma_f32_16x16x32_bf16 v[20:23], v[84:87], a[8:11], v[20:23]\n" \
  "v_exp_f32 v52, -v180\n" \
  "v_mfma_f32_16x16x32_bf16 v[20:23], v[88:91], a[16:19], v[20:23]\n" \
  "ds_read_b128 v[124:127], v131 offset:14336\n" \
  "v_add_f32 v209, 1.0, v209\n" \
  "v_mfma_f32_16x16x32_bf16 v[20:23], v[92:95], a[24:27], v[20:23]\n" \
  "s_waitcnt vmcnt(11) lgkmcnt(4)\n" \
  "s_barrier\n" \
  "v_mfma_f32_16x16x32_bf16 v[16:19], v[96:99], a[32:35], v[16:19]\n" \
  "ds_read_b128 v[64:67], v130 offset:16384\n" \
  "v_fma_f32 v52, v52, s91, s91\n" \
  "v_mfma_f32_16x16x32_bf16 v[16:19], v[100:103], a[40:43], v[16:19]\n" \
  "v_rcp_f32 v209, v209\n" \
  "v_mfma_f32_16x16x32_bf16 v[16:19], v[104:107], a[48:51], v[16:19]\n" \
  "ds_read_b128 v[68:71], v130 offset:18432\n" \
  "s_add_u32 s82, s81, s80\n" \
  "v_mfma_f32_16x16x32_bf16 v[16:19], v[108:111], a[56:59], v[16:19]\n" \
  "v_mul_f32 v208, v208, v209\n" \
  "v_mov_b32 v209, v208\n" \
  "v_mfma_f32_16x16x32_bf16 v[24:27], v[96:99], a[36:39], v[24:27]\n" \
  "ds_read_b128 v[72:75], v130 offset:20480\n" \
  "s_add_u32 m0, s68, 65536\n" \
  "v_mfma_f32_16x16x32_bf16 v[24:27], v[100:103], a[44:47], v[24:27]\n" \
  "buffer_load_dwordx4 v128, s[40:43], s82 offen offset:0 sc1 lds\n" \
  "s_add_u32 s80, s80, 16384\n" \
  "v_mfma_f32_16x16x32_bf16 v[24:27], v[104:107], a[52:55], v[24:27]\n" \
  "s_and_b32 s80, s80, 0xffff\n" \
  "v_permlane16_swap_b32 v208, v209\n" \
  "v_mfma_f32_16x16x32_bf16 v[24:27], v[108:111], a[60:63], v[24:27]\n" \
  "ds_read_b128 v[76:79], v130 offset:22528\n" \
  "s_waitcnt lgkmcnt(4)\n" \
  "v_mfma_f32_16x16x32_bf16 v[28:31], v[112:115], a[36:39], v[28:31]\n" \
  "ds_read_b128 v[80:83], v131 offset:16384\n" \
  "v_cvt_pk_bf16_f32 v208, v208, v209\n" \
  "v_mfma_f32_16x16x32_bf16 v[28:31], v[116:119], a[44:47], v[28:31]\n" \
  "v_exp_f32 v53, -v181\n" \
  "v_mfma_f32_16x16x32_bf16 v[28:31], v[120:123], a[52:55], v[28:31]\n" \
  "ds_read_b128 v[84:87], v131 offset:18432\n" \
  "v_mov_b32 v209, s71\n" \
  "v_mfma_f32_16x16x32_bf16 v[28:31], v[124:127], a[60:63], v[28:31]\n" \
  "buffer_store_dwordx2 v[208:209], v153, s[40:43], 0 offen sc1\n" \
  "v_mfma_f32_16x16x32_bf16 v[20:23], v[112:115], a[32:35], v[20:23]\n" \
  "ds_read_b128 v[88:91], v131 offset:20480\n" \
  "buffer_load_dwordx4 v128, s[40:43], s82 offen offset:1024 sc1 lds\n" \
  "v_mfma_f32_16x16x32_bf16 v[20:23], v[116:119], a[40:43], v[20:23]\n" \
  "v_exp_f32 v54, -v182\n" \
  "v_mfma_f32_16x16x32_bf16 v[20:23], v[120:123], a[48:51], v[20:23]\n" \
  "v_rcp_f32 v52, v52\n" \
  "v_mfma_f32_16x16x32_bf16 v[20:23], v[124:127], a[56:59], v[20:23]\n" \
  "ds_read_b128 v[92:95], v131 offset:22528\n" \
  "s_waitcnt lgkmcnt(4)\n" \
  "v_mfma_f32_16x16x32_bf16 v[16:19], v[64:67], a[64:67], v[16:19]\n" \
  "ds_read_b128 v[96:99], v130 offset:24576\n" \
  "v_fma_f32 v53, v53, s91, s91\n" \
  "v_mfma_f32_16x16x32_bf16 v[16:19], v[68:71], a[72:75], v[16:19]\n" \
  "v_mul_f32 v180, v180, v52\n" \
  "v_and_b32 v163, 0xffff0000, v233\n" \
  "v_mfma_f32_16x16x32_bf16 v[16:19], v[72:75], a[80:83], v[16:19]\n" \
  "ds_read_b128 v[100:103], v130 offset:26624\n" \
  "v_fma_f32 v54, v54, s91, s91\n" \
  "v_mfma_f32_16x16x32_bf16 v[16:19], v[76:79], a[88:91], v[16:19]\n" \
  "v_rcp_f32 v53, v53\n" \
  "v_mfma_f32_16x16x32_bf16 v[24:27], v[64:67], a[68:71], v[24:27]\n" \
  "ds_read_b128 v[104:107], v130 offset:28672\n" \
  "buffer_load_dwordx4 v128, s[40:43], s82 offen offset:2048 sc1 lds\n" \
  "v_mfma_f32_16x16x32_bf16 v[24:27], v[68:71], a[76:79], v[24:27]\n" \
  "v_fma_f32 v183, v163, v60, v62\n" \
  "v_mul_f32 v181, v181, v53\n" \
  "v_mfma_f32_16x16x32_bf16 v[24:27], v[72:75], a[84:87], v[24:27]\n" \
  "v_rcp_f32 v54, v54\n" \
  "v_mfma_f32_16x16x32_bf16 v[24:27], v[76:79], a[92:95], v[24:27]\n" \
  "ds_read_b128 v[108:111], v130 offset:30720\n" \
  "s_waitcnt lgkmcnt(4)\n" \
  "v_mfma_f32_16x16x32_bf16 v[28:31], v[80:83], a[68:71], v[28:31]\n" \
  "ds_read_b128 v[112:115], v131 offset:24576\n" \
  "v_lshlrev_b32 v164, 16, v234\n" \
  "v_mfma_f32_16x16x32_bf16 v[28:31], v[84:87], a[76:79], v[28:31]\n" \
  "v_exp_f32 v52, -v183\n" \
  "v_mfma_f32_16x16x32_bf16 v[28:31], v[88:91], a[84:87], v[28:31]\n" \
  "ds_read_b128 v[116:119], v131 offset:26624\n" \
  "v_mul_f32 v182, v182, v54\n" \
  "v_mfma_f32_16x16x32_bf16 v[28:31], v[92:95], a[92:95], v[28:31]\n" \
  "v_fma_f32 v184, v164, v60, v62\n" \
  "v_fma_f32 v52, v52, s91, s91\n" \
  "v_mfma_f32_16x16x32_bf16 v[20:23], v[80:83], a[64:67], v[20:23]\n" \
  "ds_read_b128 v[120:123], v131 offset:28672\n" \
  "buffer_load_dwordx4 v128, s[40:43], s82 offen offset:3072 sc1 lds\n" \
  "v_mfma_f32_16x16x32_bf16 v[20:23], v[84:87], a[72:75], v[20:23]\n" \
  "v_and_b32 v165, 0xffff0000, v234\n" \
  "v_fma_f32 v185, v165, v60, v62\n" \
  "v_mfma_f32_16x16x32_bf16 v[20:23], v[88:91], a[80:83], v[20:23]\n" \
  "ds_read_b128 v[124:127], v131 offset:30720\n" \
  "buffer_load_dword v246, v155, s[40:43], s84 offen offset:3072 sc1\n" \
  "v_mfma_f32_16x16x32_bf16 v[20:23], v[92:95], a[88:91], v[20:23]\n" \
  "s_waitcnt vmcnt(12) lgkmcnt(4)\n" \
  "s_barrier\n" \
  "v_mfma_f32_16x16x32_bf16 v[16:19], v[96:99], a[96:99], v[16:19]\n" \
  "ds_read_b128 v[64:67], v130 offset:32768\n" \
  "buffer_load_dword v247, v155, s[40:43], s85 offen offset:3072 sc1\n" \
  "v_mfma_f32_16x16x32_bf16 v[16:19], v[100:103], a[104:107], v[16:19]\n" \
  "v_rcp_f32 v52, v52\n" \
  "v_mfma_f32_16x16x32_bf16 v[16:19], v[104:107], a[112:115], v[16:19]\n" \
  "ds_read_b128 v[68:71], v130 offset:34816\n" \
  "s_add_u32 s82, s81, s80\n" \
  "v_mfma_f32_16x16x32_bf16 v[16:19], v[108:111], a[120:123], v[16:19]\n" \
  "v_exp_f32 v53, -v184\n" \
  "v_mfma_f32_16x16x32_bf16 v[24:27], v[96:99], a[100:103], v[24:27]\n" \
  "ds_read_b128 v[72:75], v130 offset:36864\n" \
  "s_add_u32 m0, s68, 81920\n" \
  "v_mfma_f32_16x16x32_bf16 v[24:27], v[100:103], a[108:111], v[24:27]\n" \
  "buffer_load_dwordx4 v128, s[40:43], s82 offen offset:0 sc1 lds\n" \
  "s_add_u32 s80, s80, 16384\n" \
  "v_mfma_f32_16x16x32_bf16 v[24:27], v[104:107], a[116:119], v[24:27]\n" \
  "s_and_b32 s80, s80, 0xffff\n" \
  "v_mul_f32 v183, v183, v52\n" \
  "v_mfma_f32_16x16x32_bf16 v[24:27], v[108:111], a[124:127], v[24:27]\n" \
  "ds_read_b128 v[76:79], v130 offset:38912\n" \
  "s_waitcnt lgkmcnt(4)\n" \
  "v_mfma_f32_16x16x32_bf16 v[28:31], v[112:115], a[100:103], v[28:31]\n" \
  "ds_read_b128 v[80:83], v131 offset:32768\n" \
  "v_fma_f32 v53, v53, s91, s91\n" \
  "v_mfma_f32_16x16x32_bf16 v[28:31], v[116:119], a[108:111], v[28:31]\n" \
  "v_exp_f32 v54, -v185\n" \
  "v_mfma_f32_16x16x32_bf16 v[28:31], v[120:123], a[116:119], v[28:31]\n" \
  "ds_read_b128 v[84:87], v131 offset:34816\n" \
  "v_lshlrev_b32 v166, 16, v235\n" \
  "v_mfma_f32_16x16x32_bf16 v[28:31], v[124:127], a[124:127], v[28:31]\n" \
  "v_rcp_f32 v53, v53\n" \
  "v_mfma_f32_16x16x32_bf16 v[20:23], v[112:115], a[96:99], v[20:23]\n" \
  "ds_read_b128 v[88:91], v131 offset:36864\n" \
  "buffer_load_dwordx4 v128, s[40:43], s82 offen offset:1024 sc1 lds\n" \
  "v_mfma_f32_16x16x32_bf16 v[20:23], v[116:119], a[104:107], v[20:23]\n" \
  "v_fma_f32 v54, v54, s91, s91\n" \
  "v_fma_f32 v186, v166, v60, v62\n" \
  "v_mfma_f32_16x16x32_bf16 v[20:23], v[120:123], a[112:115], v[20:23]\n" \
  "v_mul_f32 v184, v184, v53\n" \
  "v_and_b32 v167, 0xffff0000, v235\n" \
  "v_mfma_f32_16x16x32_bf16 v[20:23], v[124:127], a[120:123], v[20:23]\n" \
  "ds_read_b128 v[92:95], v131 offset:38912\n" \
  "s_waitcnt lgkmcnt(4)\n" \
  "v_mfma_f32_16x16x32_bf16 v[16:19], v[64:67], a[128:131], v[16:19]\n" \
  "ds_read_b128 v[96:99], v130 offset:40960\n" \
  "v_fma_f32 v187, v167, v60, v62\n" \
  "v_mfma_f32_16x16x32_bf16 v[16:19], v[68:71], a[136:139], v[16:19]\n" \
  "v_exp_f32 v52, -v186\n" \
  "v_mfma_f32_16x16x32_bf16 v[16:19], v[72:75], a[144:147], v[16:19]\n" \
  "ds_read_b128 v[100:103], v130 offset:43008\n" \
  "v_fma_f32 v52, v52, s91, s91\n" \
  "v_mfma_f32_16x16x32_bf16 v[16:19], v[76:79], a[152:155], v[16:19]\n" \
  "v_rcp_f32 v54, v54\n" \
  "v_mfma_f32_16x16x32_bf16 v[24:27], v[64:67], a[132:135], v[24:27]\n" \
  "ds_read_b128 v[104:107], v130 offset:45056\n" \
  "buffer_load_dwordx4 v128, s[40:43], s82 offen offset:2048 sc1 lds\n" \
  "v_mfma_f32_16x16x32_bf16 v[24:27], v[68:71], a[140:143], v[24:27]\n" \
  "s_waitcnt vmcnt(11)\n" \
  "buffer_store_dword v135, v50, s[40:43], s90 offen offset:3072 sc1\n" \
  "v_mfma_f32_16x16x32_bf16 v[24:27], v[72:75], a[148:151], v[24:27]\n" \
  "v_exp_f32 v53, -v187\n" \
  "v_mfma_f32_16x16x32_bf16 v[24:27], v[76:79], a[156:159], v[24:27]\n" \
  "ds_read_b128 v[108:111], v130 offset:47104\n" \
  "s_waitcnt lgkmcnt(4)\n" \
  "v_mfma_f32_16x16x32_bf16 v[28:31], v[80:83], a[132:135], v[28:31]\n" \
  "ds_read_b128 v[112:115], v131 offset:40960\n" \
  "v_mul_f32 v185, v185, v54\n" \
  "v_mfma_f32_16x16x32_bf16 v[28:31], v[84:87], a[140:143], v[28:31]\n" \
  "v_rcp_f32 v52, v52\n" \
  "v_mfma_f32_16x16x32_bf16 v[28:31], v[88:91], a[148:151], v[28:31]\n" \
  "ds_read_b128 v[116:119], v131 offset:43008\n" \
  "v_fma_f32 v53, v53, s91, s91\n" \
  "v_mfma_f32_16x16x32_bf16 v[28:31], v[92:95], a[156:159], v[28:31]\n" \
  "v_lshlrev_b32 v168, 16, v236\n" \
  "v_mul_f32 v186, v186, v52\n" \
  "v_mfma_f32_16x16x32_bf16 v[20:23], v[80:83], a[128:131], v[20:23]\n" \
  "PAD5_%=:\n" \
  "s_cbranch_execz PAD9_%=\n" \
  "ds_read_b128 v[120:123], v131 offset:45056\n" \
  "buffer_load_dwordx4 v128, s[40:43], s82 offen offset:3072 sc1 lds\n" \
  "v_mfma_f32_16x16x32_bf16 v[20:23], v[84:87], a[136:139], v[20:23]\n" \
  "v_rcp_f32 v53, v53\n" \
  "v_mfma_f32_16x16x32_bf16 v[20:23], v[88:91], a[144:147], v[20:23]\n" \
  "ds_read_b128 v[124:127], v131 offset:47104\n" \
  "v_and_b32 v169, 0xffff0000, v236\n" \
  "v_mfma_f32_16x16x32_bf16 v[20:23], v[92:95], a[152:155], v[20:23]\n" \
  "s_waitcnt vmcnt(12) lgkmcnt(4)\n" \
  "s_barrier\n" \
  "v_mfma_f32_16x16x32_bf16 v[16:19], v[96:99], a[160:163], v[16:19]\n" \
  "ds_read_b128 v[64:67], v130 offset:49152\n" \
  "v_fma_f32 v188, v168, v61, v63\n" \
  "v_mfma_f32_16x16x32_bf16 v[16:19], v[100:103], a[168:171], v[16:19]\n" \
  "v_mul_f32 v187, v187, v53\n" \
  "v_fma_f32 v189, v169, v61, v63\n" \
  "v_mfma_f32_16x16x32_bf16 v[16:19], v[104:107], a[176:179], v[16:19]\n" \
  "ds_read_b128 v[68:71], v130 offset:51200\n" \
  "s_add_u32 s82, s81, s80\n" \
  "v_mfma_f32_16x16x32_bf16 v[16:19], v[108:111], a[184:187], v[16:19]\n" \
  "v_exp_f32 v54, -v188\n" \
  "v_mfma_f32_16x16x32_bf16 v[24:27], v[96:99], a[164:167], v[24:27]\n" \
  "ds_read_b128 v[72:75], v130 offset:53248\n" \
  "s_add_u32 m0, s68, 0\n" \
  "v_mfma_f32_16x16x32_bf16 v[24:27], v[100:103], a[172:175], v[24:27]\n" \
  "buffer_load_dwordx4 v128, s[40:43], s82 offen offset:0 sc1 lds\n" \
  "s_add_u32 s80, s80, 16384\n" \
  "v_mfma_f32_16x16x32_bf16 v[24:27], v[104:107], a[180:183], v[24:27]\n" \
  "s_and_b32 s80, s80, 0xffff\n" \
  "s_waitcnt vmcnt(7)\n" \
  "v_mfma_f32_16x16x32_bf16 v[24:27], v[108:111], a[188:191], v[24:27]\n" \
  "ds_read_b128 v[76:79], v130 offset:55296\n" \
  "s_waitcnt lgkmcnt(4)\n" \
  "v_mfma_f32_16x16x32_bf16 v[28:31], v[112:115], a[164:167], v[28:31]\n" \
  "ds_read_b128 v[80:83], v131 offset:49152\n" \
  "s_waitcnt vmcnt(6)\n" \
  "v_mfma_f32_16x16x32_bf16 v[28:31], v[116:119], a[172:175], v[28:31]\n" \
  "v_cmp_eq_u32 vcc, s71, v246\n" \
  "s_cbranch_vccz I0S_%=\n" \
  "I0K_%=:\n" \
  "v_mfma_f32_16x16x32_bf16 v[28:31], v[120:123], a[180:183], v[28:31]\n" \
  "ds_read_b128 v[84:87], v131 offset:51200\n" \
  "buffer_load_dwordx4 v[200:203], v128, s[40:43], s84 offen offset:0 sc1\n" \
  "v_mfma_f32_16x16x32_bf16 v[28:31], v[124:127], a[188:191], v[28:31]\n" \
  "v_cmp_eq_u32 vcc, s71, v247\n" \
  "s_cbranch_vccz I1S_%=\n" \
  "I1K_%=:\n" \
  "v_mfma_f32_16x16x32_bf16 v[20:23], v[112:115], a[160:163], v[20:23]\n" \
  "ds_read_b128 v[88:91], v131 offset:53248\n" \
  "buffer_load_dwordx4 v128, s[40:43], s82 offen offset:1024 sc1 lds\n" \
  "v_mfma_f32_16x16x32_bf16 v[20:23], v[116:119], a[168:171], v[20:23]\n" \
  "buffer_load_dwordx4 v[204:207], v128, s[40:43], s84 offen offset:1024 sc1\n" \
  "v_lshlrev_b32 v170, 16, v237\n" \
  "v_mfma_f32_16x16x32_bf16 v[20:23], v[120:123], a[176:179], v[20:23]\n" \
  "buffer_load_dwordx4 v[208:211], v128, s[40:43], s85 offen offset:0 sc1\n" \
  "v_fma_f32 v54, v54, s91, s91\n" \
  "v_mfma_f32_16x16x32_bf16 v[20:23], v[124:127], a[184:187], v[20:23]\n" \
  "ds_read_b128 v[92:95], v131 offset:55296\n" \
  "s_waitcnt lgkmcnt(4)\n" \
  "v_mfma_f32_16x16x32_bf16 v[16:19], v[64:67], a[192:195], v[16:19]\n" \
  "ds_read_b128 v[96:99], v130 offset:57344\n" \
  "buffer_load_dwordx4 v[212:215], v128, s[40:43], s85 offen offset:1024 sc1\n" \
  "v_mfma_f32_16x16x32_bf16 v[16:19], v[68:71], a[200:203], v[16:19]\n" \
  "v_exp_f32 v52, -v189\n" \
  "v_mfma_f32_16x16x32_bf16 v[16:19], v[72:75], a[208:211], v[16:19]\n" \
  "ds_read_b128 v[100:103], v130 offset:59392\n" \
  "v_fma_f32 v190, v170, v61, v63\n" \
  "v_mfma_f32_16x16x32_bf16 v[16:19], v[76:79], a[216:219], v[16:19]\n" \
  "v_rcp_f32 v54, v54\n" \
  "v_mfma_f32_16x16x32_bf16 v[24:27], v[64:67], a[196:199], v[24:27]\n" \
  "ds_read_b128 v[104:107], v130 offset:61440\n" \
  "buffer_load_dwordx4 v128, s[40:43], s82 offen offset:2048 sc1 lds\n" \
  "v_mfma_f32_16x16x32_bf16 v[24:27], v[68:71], a[204:207], v[24:27]\n" \
  "v_fma_f32 v52, v52, s91, s91\n" \
  "v_mul_f32 v188, v188, v54\n" \
  "v_mfma_f32_16x16x32_bf16 v[24:27], v[72:75], a[212:215], v[24:27]\n" \
  "v_exp_f32 v53, -v190\n" \
  "v_mfma_f32_16x16x32_bf16 v[24:27], v[76:79], a[220:223], v[24:27]\n" \
  "ds_read_b128 v[108:111], v130 offset:63488\n" \
  "s_waitcnt lgkmcnt(4)\n" \
  "v_mfma_f32_16x16x32_bf16 v[28:31], v[80:83], a[196:199], v[28:31]\n" \
  "ds_read_b128 v[112:115], v131 offset:57344\n" \
  "v_and_b32 v171, 0xffff0000, v237\n" \
  "v_mfma_f32_16x16x32_bf16 v[28:31], v[84:87], a[204:207], v[28:31]\n" \
  "v_rcp_f32 v52, v52\n" \
  "v_mfma_f32_16x16x32_bf16 v[28:31], v[88:91], a[212:215], v[28:31]\n" \
  "ds_read_b128 v[116:119], v131 offset:59392\n" \
  "v_fma_f32 v53, v53, s91, s91\n" \
  "v_mfma_f32_16x16x32_bf16 v[28:31], v[92:95], a[220:223], v[28:31]\n" \
  "v_fma_f32 v191, v171, v61, v63\n" \
  "v_mul_f32 v189, v189, v52\n" \
  "v_mfma_f32_16x16x32_bf16 v[20:23], v[80:83], a[192:195], v[20:23]\n" \
  "ds_read_b128 v[120:123], v131 offset:61440\n" \
  "buffer_load_dwordx4 v128, s[40:43], s82 offen offset:3072 sc1 lds\n" \
  "v_mfma_f32_16x16x32_bf16 v[20:23], v[84:87], a[200:203], v[20:23]\n" \
  "v_rcp_f32 v53, v53\n" \
  "v_mfma_f32_16x16x32_bf16 v[20:23], v[88:91], a[208:211], v[20:23]\n" \
  "ds_read_b128 v[124:127], v131 offset:63488\n" \
  "v_lshlrev_b32 v172, 16, v238\n" \
  "v_mfma_f32_16x16x32_bf16 v[20:23], v[92:95], a[216:219], v[20:23]\n" \
  "s_waitcnt lgkmcnt(4)\n" \
  "s_barrier\n" \
  "v_mfma_f32_16x16x32_bf16 v[16:19], v[96:99], a[224:227], v[16:19]\n" \
  "ds_read_b128 v[64:67], v129 offset:0\n" \
  "v_mul_f32 v190, v190, v53\n" \
  "v_mfma_f32_16x16x32_bf16 v[16:19], v[100:103], a[232:235], v[16:19]\n" \
  "v_exp_f32 v54, -v191\n" \
  "v_mfma_f32_16x16x32_bf16 v[16:19], v[104:107], a[240:243], v[16:19]\n" \
  "ds_read_b128 v[68:71], v129 offset:2048\n" \
  "s_add_u32 s82, s81, s80\n" \
  "v_mfma_f32_16x16x32_bf16 v[16:19], v[108:111], a[248:251], v[16:19]\n" \
  "v_fma_f32 v192, v172, v61, v63\n" \
  "v_and_b32 v173, 0xffff0000, v238\n" \
  "v_mfma_f32_16x16x32_bf16 v[24:27], v[96:99], a[228:231], v[24:27]\n" \
  "ds_read_b128 v[72:75], v129 offset:4096\n" \
  "s_add_u32 m0, s68, 16384\n" \
  "v_mfma_f32_16x16x32_bf16 v[24:27], v[100:103], a[236:239], v[24:27]\n" \
  "buffer_load_dwordx4 v128, s[40:43], s82 offen offset:0 sc1 lds\n" \
  "s_add_u32 s80, s80, 16384\n" \
  "v_mfma_f32_16x16x32_bf16 v[24:27], v[104:107], a[244:247], v[24:27]\n" \
  "s_and_b32 s80, s80, 0xffff\n" \
  "v_fma_f32 v54, v54, s91, s91\n" \
  "v_mfma_f32_16x16x32_bf16 v[24:27], v[108:111], a[252:255], v[24:27]\n" \
  "ds_read_b128 v[76:79], v129 offset:6144\n" \
  "s_waitcnt lgkmcnt(4)\n" \
  "v_mfma_f32_16x16x32_bf16 v[28:31], v[112:115], a[228:231], v[28:31]\n" \
  "ds_read_b128 v[80:83], v129 offset:1024\n" \
  "v_fma_f32 v193, v173, v61, v63\n" \
  "v_mfma_f32_16x16x32_bf16 v[28:31], v[116:119], a[236:239], v[28:31]\n" \
  "v_exp_f32 v52, -v192\n" \
  "v_mfma_f32_16x16x32_bf16 v[28:31], v[120:123], a[244:247], v[28:31]\n" \
  "ds_read_b128 v[84:87], v129 offset:3072\n" \
  "v_fma_f32 v52, v52, s91, s91\n" \
  "v_mfma_f32_16x16x32_bf16 v[28:31], v[124:127], a[252:255], v[28:31]\n" \
  "v_rcp_f32 v54, v54\n" \
  "v_mfma_f32_16x16x32_bf16 v[20:23], v[112:115], a[224:227], v[20:23]\n" \
  "ds_read_b128 v[88:91], v129 offset:5120\n" \
  "buffer_load_dwordx4 v128, s[40:43], s82 offen offset:1024 sc1 lds\n" \
  "v_mfma_f32_16x16x32_bf16 v[20:23], v[116:119], a[232:235], v[20:23]\n" \
  "v_cvt_pk_bf16_f32 v40, v24, v25\n" \
  "v_cvt_pk_bf16_f32 v42, v26, v27\n" \
  "v_mfma_f32_16x16x32_bf16 v[20:23], v[120:123], a[240:243], v[20:23]\n" \
  "buffer_store_dwordx4 v[40:43], v128, s[40:43], s89 offen offset:2048 sc1\n" \
  "v_mfma_f32_16x16x32_bf16 v[20:23], v[124:127], a[248:251], v[20:23]\n" \
  "ds_read_b128 v[92:95], v129 offset:7168\n" \
  "s_waitcnt lgkmcnt(4)\n" \
  "v_mfma_f32_16x16x32_bf16 v[0:3], v[64:67], a[0:3], 0\n" \
  "ds_read_b128 v[96:99], v129 offset:8192\n" \
  "v_mul_f32 v191, v191, v54\n" \
  "v_mfma_f32_16x16x32_bf16 v[0:3], v[68:71], a[8:11], v[0:3]\n" \
  "v_cvt_pk_bf16_f32 v44, v28, v29\n" \
  "v_cvt_pk_bf16_f32 v46, v30, v31\n" \
  "v_mfma_f32_16x16x32_bf16 v[0:3], v[72:75], a[16:19], v[0:3]\n" \
  "ds_read_b128 v[100:103], v129 offset:10240\n" \
  "v_lshlrev_b32 v174, 16, v239\n" \
  "v_mfma_f32_16x16x32_bf16 v[0:3], v[76:79], a[24:27], v[0:3]\n" \
  "buffer_store_dwordx4 v[44:47], v128, s[40:43], s90 offen offset:2048 sc1\n" \
  "v_mfma_f32_16x16x32_bf16 v[8:11], v[64:67], a[4:7], 0\n" \
  "ds_read_b128 v[104:107], v129 offset:12288\n" \
  "buffer_load_dwordx4 v128, s[40:43], s82 offen offset:2048 sc1 lds\n" \
  "v_mfma_f32_16x16x32_bf16 v[8:11], v[68:71], a[12:15], v[8:11]\n" \
  "v_cvt_pk_bf16_f32 v36, v20, v21\n" \
  "v_cvt_pk_bf16_f32 v38, v22, v23\n" \
  "v_mfma_f32_16x16x32_bf16 v[8:11], v[72:75], a[20:23], v[8:11]\n" \
  "buffer_store_dwordx4 v[36:39], v128, s[40:43], s88 offen offset:2048 sc1\n" \
  "v_mfma_f32_16x16x32_bf16 v[8:11], v[76:79], a[28:31], v[8:11]\n" \
  "ds_read_b128 v[108:111], v129 offset:14336\n" \
  "s_waitcnt lgkmcnt(4)\n" \
  "v_mfma_f32_16x16x32_bf16 v[12:15], v[80:83], a[4:7], 0\n" \
  "ds_read_b128 v[112:115], v129 offset:9216\n" \
  "v_fma_f32 v194, v174, v61, v63\n" \
  "v_mfma_f32_16x16x32_bf16 v[12:15], v[84:87], a[12:15], v[12:15]\n" \
  "v_exp_f32 v53, -v193\n" \
  "v_mfma_f32_16x16x32_bf16 v[12:15], v[88:91], a[20:23], v[12:15]\n" \
  "ds_read_b128 v[116:119], v129 offset:11264\n" \
  "v_fma_f32 v53, v53, s91, s91\n" \
  "v_mfma_f32_16x16x32_bf16 v[12:15], v[92:95], a[28:31], v[12:15]\n" \
  "v_rcp_f32 v52, v52\n" \
  "v_mfma_f32_16x16x32_bf16 v[4:7], v[80:83], a[0:3], 0\n" \
  "ds_read_b128 v[120:123], v129 offset:13312\n" \
  "buffer_load_dwordx4 v128, s[40:43], s82 offen offset:3072 sc1 lds\n" \
  "v_mfma_f32_16x16x32_bf16 v[4:7], v[84:87], a[8:11], v[4:7]\n" \
  "v_exp_f32 v54, -v194\n" \
  "v_mfma_f32_16x16x32_bf16 v[4:7], v[88:91], a[16:19], v[4:7]\n" \
  "ds_read_b128 v[124:127], v129 offset:15360\n" \
  "v_mul_f32 v192, v192, v52\n" \
  "v_mfma_f32_16x16x32_bf16 v[4:7], v[92:95], a[24:27], v[4:7]\n" \
  "s_waitcnt vmcnt(15) lgkmcnt(4)\n" \
  "s_barrier\n" \
  "v_mfma_f32_16x16x32_bf16 v[0:3], v[96:99], a[32:35], v[0:3]\n" \
  "ds_read_b128 v[64:67], v129 offset:16384\n" \
  "v_fma_f32 v54, v54, s91, s91\n" \
  "v_mfma_f32_16x16x32_bf16 v[0:3], v[100:103], a[40:43], v[0:3]\n" \
  "v_rcp_f32 v53, v53\n" \
  "v_mfma_f32_16x16x32_bf16 v[0:3], v[104:107], a[48:51], v[0:3]\n" \
  "ds_read_b128 v[68:71], v129 offset:18432\n" \
  "v_and_b32 v175, 0xffff0000, v239\n" \
  "v_mfma_f32_16x16x32_bf16 v[0:3], v[108:111], a[56:59], v[0:3]\n" \
  "v_rcp_f32 v54, v54\n" \
  "v_mfma_f32_16x16x32_bf16 v[8:11], v[96:99], a[36:39], v[8:11]\n" \
  "ds_read_b128 v[72:75], v129 offset:20480\n" \
  "v_mul_f32 v193, v193, v53\n" \
  "v_mfma_f32_16x16x32_bf16 v[8:11], v[100:103], a[44:47], v[8:11]\n" \
  "v_fma_f32 v195, v175, v61, v63\n" \
  "v_mul_f32 v194, v194, v54\n" \
  "v_mfma_f32_16x16x32_bf16 v[8:11], v[104:107], a[52:55], v[8:11]\n" \
  "v_lshlrev_b32 v176, 16, v240\n" \
  "v_and_b32 v177, 0xffff0000, v240\n" \
  "v_mfma_f32_16x16x32_bf16 v[8:11], v[108:111], a[60:63], v[8:11]\n" \
  "ds_read_b128 v[76:79], v129 offset:22528\n" \
  "s_waitcnt lgkmcnt(4)\n" \
  "v_mfma_f32_16x16x32_bf16 v[12:15], v[112:115], a[36:39], v[12:15]\n" \
  "ds_read_b128 v[80:83], v129 offset:17408\n" \
  "v_fma_f32 v196, v176, v60, v62\n" \
  "v_mfma_f32_16x16x32_bf16 v[12:15], v[116:119], a[44:47], v[12:15]\n" \
  "v_exp_f32 v52, -v195\n" \
  "v_mfma_f32_16x16x32_bf16 v[12:15], v[120:123], a[52:55], v[12:15]\n" \
  "ds_read_b128 v[84:87], v129 offset:19456\n" \
  "v_fma_f32 v197, v177, v60, v62\n" \
  "v_mfma_f32_16x16x32_bf16 v[12:15], v[124:127], a[60:63], v[12:15]\n" \
  "v_exp_f32 v53, -v196\n" \
  "v_mfma_f32_16x16x32_bf16 v[4:7], v[112:115], a[32:35], v[4:7]\n" \
  "ds_read_b128 v[88:91], v129 offset:21504\n" \
  "buffer_load_dword v246, v155, s[40:43], s86 offen offset:3072 sc1\n" \
  "v_mfma_f32_16x16x32_bf16 v[4:7], v[116:119], a[40:43], v[4:7]\n" \
  "v_fma_f32 v52, v52, s91, s91\n" \
  "v_fma_f32 v53, v53, s91, s91\n" \
  "v_mfma_f32_16x16x32_bf16 v[4:7], v[120:123], a[48:51], v[4:7]\n" \
  "v_exp_f32 v54, -v197\n" \
  "v_mfma_f32_16x16x32_bf16 v[4:7], v[124:127], a[56:59], v[4:7]\n" \
  "ds_read_b128 v[92:95], v129 offset:23552\n" \
  "s_waitcnt lgkmcnt(4)\n" \
  "v_mfma_f32_16x16x32_bf16 v[0:3], v[64:67], a[64:67], v[0:3]\n" \
  "ds_read_b128 v[96:99], v129 offset:24576\n" \
  "v_fma_f32 v54, v54, s91, s91\n" \
  "v_mfma_f32_16x16x32_bf16 v[0:3], v[68:71], a[72:75], v[0:3]\n" \
  "v_rcp_f32 v52, v52\n" \
  "v_mfma_f32_16x16x32_bf16 v[0:3], v[72:75], a[80:83], v[0:3]\n" \
  "ds_read_b128 v[100:103], v129 offset:26624\n" \
  "buffer_load_dwordx4 v[32:35], v149, s[40:43], 0 offen offset:0 sc1\n" \
  "v_mfma_f32_16x16x32_bf16 v[0:3], v[76:79], a[88:91], v[0:3]\n" \
  "v_rcp_f32 v53, v53\n" \
  "v_mfma_f32_16x16x32_bf16 v[8:11], v[64:67], a[68:71], v[8:11]\n" \
  "ds_read_b128 v[104:107], v129 offset:28672\n" \
  "buffer_load_dwordx4 v[36:39], v149, s[40:43], 0 offen offset:16 sc1\n" \
  "v_mfma_f32_16x16x32_bf16 v[8:11], v[68:71], a[76:79], v[8:11]\n" \
  "v_rcp_f32 v54, v54\n" \
  "v_mfma_f32_16x16x32_bf16 v[8:11], v[72:75], a[84:87], v[8:11]\n" \
  "buffer_load_dwordx4 v[40:43], v150, s[40:43], 0 offen offset:0 sc1\n" \
  "v_mul_f32 v195, v195, v52\n" \
  "v_mfma_f32_16x16x32_bf16 v[8:11], v[76:79], a[92:95], v[8:11]\n" \
  "ds_read_b128 v[108:111], v129 offset:30720\n" \
  "s_waitcnt lgkmcnt(4)\n" \
  "v_mfma_f32_16x16x32_bf16 v[12:15], v[80:83], a[68:71], v[12:15]\n" \
  "ds_read_b128 v[112:115], v129 offset:25600\n" \
  "buffer_load_dwordx4 v[44:47], v150, s[40:43], 0 offen offset:16 sc1\n" \
  "v_mfma_f32_16x16x32_bf16 v[12:15], v[84:87], a[76:79], v[12:15]\n" \
  "v_mul_f32 v196, v196, v53\n" \
  "v_mul_f32 v197, v197, v54\n" \
  "v_mfma_f32_16x16x32_bf16 v[12:15], v[88:91], a[84:87], v[12:15]\n" \
  "ds_read_b128 v[116:119], v129 offset:27648\n" \
  "buffer_load_dwordx4 v[48:51], v151, s[40:43], 0 offen offset:0 sc1\n" \
  "v_mfma_f32_16x16x32_bf16 v[12:15], v[92:95], a[92:95], v[12:15]\n" \
  "v_and_b32 v179, 0xffff0000, v241\n" \
  "v_fma_f32 v199, v179, v60, v62\n" \
  "v_mfma_f32_16x16x32_bf16 v[4:7], v[80:83], a[64:67], v[4:7]\n" \
  "ds_read_b128 v[120:123], v129 offset:29696\n" \
  "v_lshlrev_b32 v178, 16, v241\n" \
  "v_mfma_f32_16x16x32_bf16 v[4:7], v[84:87], a[72:75], v[4:7]\n" \
  "v_exp_f32 v53, -v199\n" \
  "v_mfma_f32_16x16x32_bf16 v[4:7], v[88:91], a[80:83], v[4:7]\n" \
  "ds_read_b128 v[124:127], v129 offset:31744\n" \
  "v_fma_f32 v198, v178, v60, v62\n" \
  "v_mfma_f32_16x16x32_bf16 v[4:7], v[92:95], a[88:91], v[4:7]\n" \
  "s_waitcnt vmcnt(13) lgkmcnt(4)\n" \
  "s_barrier\n" \
  "v_mfma_f32_16x16x32_bf16 v[0:3], v[96:99], a[96:99], v[0:3]\n" \
  "ds_read_b128 v[64:67], v128 offset:0\n" \
  "v_fma_f32 v53, v53, s91, s91\n" \
  "v_mfma_f32_16x16x32_bf16 v[0:3], v[100:103], a[104:107], v[0:3]\n" \
  "v_exp_f32 v52, -v198\n" \
  "v_mfma_f32_16x16x32_bf16 v[0:3], v[104:107], a[112:115], v[0:3]\n" \
  "ds_read_b128 v[68:71], v128 offset:2048\n" \
  "v_fma_f32 v52, v52, s91, s91\n" \
  "v_mfma_f32_16x16x32_bf16 v[0:3], v[108:111], a[120:123], v[0:3]\n" \
  "v_rcp_f32 v53, v53\n" \
  "v_mfma_f32_16x16x32_bf16 v[8:11], v[96:99], a[100:103], v[8:11]\n" \
  "ds_read_b128 v[72:75], v128 offset:4096\n" \
  "v_mul_f32 v199, v199, v53\n" \
  "v_mfma_f32_16x16x32_bf16 v[8:11], v[100:103], a[108:111], v[8:11]\n" \
  "v_rcp_f32 v52, v52\n" \
  "v_mfma_f32_16x16x32_bf16 v[8:11], v[104:107], a[116:119], v[8:11]\n" \
  "v_mul_f32 v198, v198, v52\n" \
  "v_mfma_f32_16x16x32_bf16 v[8:11], v[108:111], a[124:127], v[8:11]\n" \
  "ds_read_b128 v[76:79], v128 offset:6144\n" \
  "s_waitcnt lgkmcnt(4)\n" \
  "v_mfma_f32_16x16x32_bf16 v[12:15], v[112:115], a[100:103], v[12:15]\n" \
  "ds_read_b128 v[80:83], v128 offset:1024\n" \
  "v_mov_b32 v232, 0\n" \
  "v_mfma_f32_16x16x32_bf16 v[12:15], v[116:119], a[108:111], v[12:15]\n" \
  "v_mov_b32 v233, 0\n" \
  "v_mov_b32 v234, v156\n" \
  "v_mfma_f32_16x16x32_bf16 v[12:15], v[120:123], a[116:119], v[12:15]\n" \
  "ds_read_b128 v[84:87], v128 offset:3072\n" \
  "v_mov_b32 v235, v158\n" \
  "v_mfma_f32_16x16x32_bf16 v[12:15], v[124:127], a[124:127], v[12:15]\n" \
  "v_mov_b32 v236, 0\n" \
  "v_mov_b32 v237, 0\n" \
  "v_mfma_f32_16x16x32_bf16 v[4:7], v[112:115], a[96:99], v[4:7]\n" \
  "ds_read_b128 v[88:91], v128 offset:5120\n" \
  "v_mfma_f32_16x16x32_bf16 v[4:7], v[116:119], a[104:107], v[4:7]\n" \
  "v_mfma_f32_16x16x32_bf16 v[4:7], v[120:123], a[112:115], v[4:7]\n" \
  "v_mfma_f32_16x16x32_bf16 v[4:7], v[124:127], a[120:123], v[4:7]\n" \
  "ds_read_b128 v[92:95], v128 offset:7168\n" \
  "s_waitcnt lgkmcnt(4)\n" \
  "v_mfma_f32_16x16x32_bf16 v[0:3], v[64:67], a[128:131], v[0:3]\n" \
  "ds_read_b128 v[96:99], v128 offset:8192\n" \
  "v_mfma_f32_16x16x32_bf16 v[0:3], v[68:71], a[136:139], v[0:3]\n" \
  "v_mfma_f32_16x16x32_bf16 v[0:3], v[72:75], a[144:147], v[0:3]\n" \
  "ds_read_b128 v[100:103], v128 offset:10240\n" \
  "v_mfma_f32_16x16x32_bf16 v[0:3], v[76:79], a[152:155], v[0:3]\n" \
  "v_mfma_f32_16x16x32_bf16 v[8:11], v[64:67], a[132:135], v[8:11]\n" \
  "ds_read_b128 v[104:107], v128 offset:12288\n" \
  "s_waitcnt vmcnt(5)\n" \
  "v_mfma_f32_16x16x32_bf16 v[8:11], v[68:71], a[140:143], v[8:11]\n" \
  "v_cmp_eq_u32 vcc, s71, v246\n" \
  "s_cbranch_vccz I2S_%=\n" \
  "I2K_%=:\n" \
  "v_mfma_f32_16x16x32_bf16 v[8:11], v[72:75], a[148:151], v[8:11]\n" \
  "buffer_load_dwordx4 v[216:219], v128, s[40:43], s86 offen offset:0 sc1\n" \
  "v_mfma_f32_16x16x32_bf16 v[8:11], v[76:79], a[156:159], v[8:11]\n" \
  "ds_read_b128 v[108:111], v128 offset:14336\n" \
  "s_waitcnt lgkmcnt(4)\n" \
  "v_mfma_f32_16x16x32_bf16 v[12:15], v[80:83], a[132:135], v[12:15]\n" \
  "ds_read_b128 v[112:115], v128 offset:9216\n" \
  "buffer_load_dwordx4 v[220:223], v128, s[40:43], s86 offen offset:1024 sc1\n" \
  "v_mfma_f32_16x16x32_bf16 v[12:15], v[84:87], a[140:143], v[12:15]\n" \
  "v_mfma_f32_16x16x32_bf16 v[12:15], v[88:91], a[148:151], v[12:15]\n" \
  "ds_read_b128 v[116:119], v128 offset:11264\n" \
  "v_mfma_f32_16x16x32_bf16 v[12:15], v[92:95], a[156:159], v[12:15]\n" \
  "v_mfma_f32_16x16x32_bf16 v[4:7], v[80:83], a[128:131], v[4:7]\n" \
  "ds_read_b128 v[120:123], v128 offset:13312\n" \
  "v_mfma_f32_16x16x32_bf16 v[4:7], v[84:87], a[136:139], v[4:7]\n" \
  "v_mfma_f32_16x16x32_bf16 v[4:7], v[88:91], a[144:147], v[4:7]\n" \
  "ds_read_b128 v[124:127], v128 offset:15360\n" \
  "v_mfma_f32_16x16x32_bf16 v[4:7], v[92:95], a[152:155], v[4:7]\n" \
  "s_waitcnt lgkmcnt(4)\n" \
  "s_barrier\n" \
  "v_mfma_f32_16x16x32_bf16 v[0:3], v[96:99], a[160:163], v[0:3]\n" \
  "ds_read_b128 v[64:67], v128 offset:16384\n" \
  "v_mfma_f32_16x16x32_bf16 v[0:3], v[100:103], a[168:171], v[0:3]\n" \
  "v_mfma_f32_16x16x32_bf16 v[0:3], v[104:107], a[176:179], v[0:3]\n" \
  "ds_read_b128 v[68:71], v128 offset:18432\n" \
  "v_mfma_f32_16x16x32_bf16 v[0:3], v[108:111], a[184:187], v[0:3]\n" \
  "v_mfma_f32_16x16x32_bf16 v[8:11], v[96:99], a[164:167], v[8:11]\n" \
  "ds_read_b128 v[72:75], v128 offset:20480\n" \
  "v_mfma_f32_16x16x32_bf16 v[8:11], v[100:103], a[172:175], v[8:11]\n" \
  "v_mfma_f32_16x16x32_bf16 v[8:11], v[104:107], a[180:183], v[8:11]\n" \
  "v_mfma_f32_16x16x32_bf16 v[8:11], v[108:111], a[188:191], v[8:11]\n" \
  "ds_read_b128 v[76:79], v128 offset:22528\n" \
  "s_waitcnt lgkmcnt(4)\n" \
  "v_mfma_f32_16x16x32_bf16 v[12:15], v[112:115], a[164:167], v[12:15]\n" \
  "ds_read_b128 v[80:83], v128 offset:17408\n" \
  "v_mfma_f32_16x16x32_bf16 v[12:15], v[116:119], a[172:175], v[12:15]\n" \
  "v_mfma_f32_16x16x32_bf16 v[12:15], v[120:123], a[180:183], v[12:15]\n" \
  "ds_read_b128 v[84:87], v128 offset:19456\n" \
  "v_mfma_f32_16x16x32_bf16 v[12:15], v[124:127], a[188:191], v[12:15]\n" \
  "v_mfma_f32_16x16x32_bf16 v[4:7], v[112:115], a[160:163], v[4:7]\n" \
  "ds_read_b128 v[88:91], v128 offset:21504\n" \
  "v_mfma_f32_16x16x32_bf16 v[4:7], v[116:119], a[168:171], v[4:7]\n" \
  "buffer_load_dwordx4 v[20:23], v128, s[40:43], s84 offen offset:2048 sc1\n" \
  "v_mfma_f32_16x16x32_bf16 v[4:7], v[120:123], a[176:179], v[4:7]\n" \
  "v_mfma_f32_16x16x32_bf16 v[4:7], v[124:127], a[184:187], v[4:7]\n" \
  "ds_read_b128 v[92:95], v128 offset:23552\n" \
  "s_waitcnt lgkmcnt(4)\n" \
  "v_mfma_f32_16x16x32_bf16 v[0:3], v[64:67], a[192:195], v[0:3]\n" \
  "ds_read_b128 v[96:99], v128 offset:24576\n" \
  "v_mfma_f32_16x16x32_bf16 v[0:3], v[68:71], a[200:203], v[0:3]\n" \
  "v_mfma_f32_16x16x32_bf16 v[0:3], v[72:75], a[208:211], v[0:3]\n" \
  "ds_read_b128 v[100:103], v128 offset:26624\n" \
  "buffer_load_dwordx4 v[24:27], v128, s[40:43], s85 offen offset:2048 sc1\n" \
  "v_mfma_f32_16x16x32_bf16 v[0:3], v[76:79], a[216:219], v[0:3]\n" \
  "v_mfma_f32_16x16x32_bf16 v[8:11], v[64:67], a[196:199], v[8:11]\n" \
  "ds_read_b128 v[104:107], v128 offset:28672\n" \
  "v_mfma_f32_16x16x32_bf16 v[8:11], v[68:71], a[204:207], v[8:11]\n" \
  "v_mfma_f32_16x16x32_bf16 v[8:11], v[72:75], a[212:215], v[8:11]\n" \
  "buffer_load_dwordx4 v[28:31], v128, s[40:43], s86 offen offset:2048 sc1\n" \
  "s_waitcnt vmcnt(5)\n" \
  "v_mfma_f32_16x16x32_bf16 v[8:11], v[76:79], a[220:223], v[8:11]\n" \
  "ds_read_b128 v[108:111], v128 offset:30720\n" \
  "s_waitcnt lgkmcnt(4)\n" \
  "v_mfma_f32_16x16x32_bf16 v[12:15], v[80:83], a[196:199], v[12:15]\n" \
  "ds_read_b128 v[112:115], v128 offset:25600\n" \
  "v_xor_b32 v52, s71, v33\n" \
  "v_mfma_f32_16x16x32_bf16 v[12:15], v[84:87], a[204:207], v[12:15]\n" \
  "v_bitop3_b32 v52, v35, v52, s71 bitop3:0xde\n" \
  "v_bitop3_b32 v52, v37, v52, s71 bitop3:0xde\n" \
  "v_mfma_f32_16x16x32_bf16 v[12:15], v[88:91], a[212:215], v[12:15]\n" \
  "ds_read_b128 v[116:119], v128 offset:27648\n" \
  "v_bitop3_b32 v52, v39, v52, s71 bitop3:0xde\n" \
  "v_mfma_f32_16x16x32_bf16 v[12:15], v[92:95], a[220:223], v[12:15]\n" \
  "v_bitop3_b32 v52, v49, v52, s71 bitop3:0xde\n" \
  "v_bitop3_b32 v52, v51, v52, s71 bitop3:0xde\n" \
  "v_mfma_f32_16x16x32_bf16 v[4:7], v[80:83], a[192:195], v[4:7]\n" \
  "ds_read_b128 v[120:123], v128 offset:29696\n" \
  "v_xor_b32 v53, s71, v41\n" \
  "v_mfma_f32_16x16x32_bf16 v[4:7], v[84:87], a[200:203], v[4:7]\n" \
  "v_bitop3_b32 v53, v43, v53, s71 bitop3:0xde\n" \
  "v_bitop3_b32 v53, v45, v53, s71 bitop3:0xde\n" \
  "v_mfma_f32_16x16x32_bf16 v[4:7], v[88:91], a[208:211], v[4:7]\n" \
  "ds_read_b128 v[124:127], v128 offset:31744\n" \
  "v_bitop3_b32 v53, v47, v53, s71 bitop3:0xde\n" \
  "v_mfma_f32_16x16x32_bf16 v[4:7], v[92:95], a[216:219], v[4:7]\n" \
  "v_cndmask_b32 v52, 0, v52, s[92:93]\n" \
  "s_waitcnt lgkmcnt(4)\n" \
  "v_mfma_f32_16x16x32_bf16 v[0:3], v[96:99], a[224:227], v[0:3]\n" \
  "v_cndmask_b32 v53, 0, v53, s[94:95]\n" \
  "v_or_b32 v52, v52, v53\n" \
  "v_mfma_f32_16x16x32_bf16 v[0:3], v[100:103], a[232:235], v[0:3]\n" \
  "v_cmp_ne_u32 vcc, 0, v52\n" \
  "s_cbranch_vccnz GTS_%=\n" \
  "GTK_%=:\n" \
  "v_mfma_f32_16x16x32_bf16 v[0:3], v[104:107], a[240:243], v[0:3]\n" \
  "s_mov_b32 s60, 0xffff0000\n" \
  "v_lshl_or_b32 v180, v32, 16, v180\n" \
  "v_mfma_f32_16x16x32_bf16 v[0:3], v[108:111], a[248:251], v[0:3]\n" \
  "v_and_or_b32 v181, v32, s60, v181\n" \
  "s_waitcnt vmcnt(3)\n" \
  "v_mfma_f32_16x16x32_bf16 v[0:3], v[234:237], v[200:203], v[0:3]\n" \
  "v_lshl_or_b32 v182, v34, 16, v182\n" \
  "v_and_or_b32 v183, v34, s60, v183\n" \
  "v_mfma_f32_16x16x32_bf16 v[0:3], v[234:237], v[208:211], v[0:3]\n" \
  "v_lshl_or_b32 v184, v36, 16, v184\n" \
  "v_and_or_b32 v185, v36, s60, v185\n" \
  "v_mfma_f32_16x16x32_bf16 v[0:3], v[234:237], v[216:219], v[0:3]\n" \
  "v_lshl_or_b32 v186, v38, 16, v186\n" \
  "v_and_or_b32 v187, v38, s60, v187\n" \
  "v_mfma_f32_16x16x32_bf16 v[8:11], v[96:99], a[228:231], v[8:11]\n" \
  "v_lshl_or_b32 v188, v40, 16, v188\n" \
  "v_and_or_b32 v189, v40, s60, v189\n" \
  "v_mfma_f32_16x16x32_bf16 v[8:11], v[100:103], a[236:239], v[8:11]\n" \
  "v_lshl_or_b32 v190, v42, 16, v190\n" \
  "v_and_or_b32 v191, v42, s60, v191\n" \
  "v_mfma_f32_16x16x32_bf16 v[8:11], v[104:107], a[244:247], v[8:11]\n" \
  "v_lshl_or_b32 v192, v44, 16, v192\n" \
  "v_and_or_b32 v193, v44, s60, v193\n" \
  "v_mfma_f32_16x16x32_bf16 v[8:11], v[108:111], a[252:255], v[8:11]\n" \
  "v_lshl_or_b32 v194, v46, 16, v194\n" \
  "v_and_or_b32 v195, v46, s60, v195\n" \
  "v_mfma_f32_16x16x32_bf16 v[8:11], v[234:237], v[204:207], v[8:11]\n" \
  "v_lshl_or_b32 v196, v48, 16, v196\n" \
  "v_and_or_b32 v197, v48, s60, v197\n" \
  "v_mfma_f32_16x16x32_bf16 v[8:11], v[234:237], v[212:215], v[8:11]\n" \
  "v_lshl_or_b32 v198, v50, 16, v198\n" \
  "v_and_or_b32 v199, v50, s60, v199\n" \
  "v_mfma_f32_16x16x32_bf16 v[8:11], v[234:237], v[220:223], v[8:11]\n" \
  "s_waitcnt lgkmcnt(0)\n" \
  "v_mfma_f32_16x16x32_bf16 v[12:15], v[112:115], a[228:231], v[12:15]\n" \
  "v_mfma_f32_16x16x32_bf16 v[12:15], v[116:119], a[236:239], v[12:15]\n" \
  "v_mfma_f32_16x16x32_bf16 v[12:15], v[120:123], a[244:247], v[12:15]\n" \
  "v_mfma_f32_16x16x32_bf16 v[12:15], v[124:127], a[252:255], v[12:15]\n" \
  "v_mfma_f32_16x16x32_bf16 v[12:15], v[232:235], v[204:207], v[12:15]\n" \
  "v_mfma_f32_16x16x32_bf16 v[12:15], v[232:235], v[212:215], v[12:15]\n" \
  "v_mfma_f32_16x16x32_bf16 v[12:15], v[232:235], v[220:223], v[12:15]\n" \
  "v_mfma_f32_16x16x32_bf16 v[4:7], v[112:115], a[224:227], v[4:7]\n" \
  "v_mfma_f32_16x16x32_bf16 v[4:7], v[116:119], a[232:235], v[4:7]\n" \
  "v_mfma_f32_16x16x32_bf16 v[4:7], v[120:123], a[240:243], v[4:7]\n" \
  "v_mfma_f32_16x16x32_bf16 v[4:7], v[124:127], a[248:251], v[4:7]\n" \
  "v_mfma_f32_16x16x32_bf16 v[4:7], v[232:235], v[200:203], v[4:7]\n" \
  "v_mfma_f32_16x16x32_bf16 v[4:7], v[232:235], v[208:211], v[4:7]\n" \
  "v_mfma_f32_16x16x32_bf16 v[4:7], v[232:235], v[216:219], v[4:7]\n" \
  "v_pk_fma_f32 v[8:9], v[8:9], v[188:189], v[168:169]\n" \
  "v_cvt_pk_bf16_f32 v68, v8, v9\n" \
  "v_pk_fma_f32 v[10:11], v[10:11], v[190:191], v[170:171]\n" \
  "v_cvt_pk_bf16_f32 v69, v10, v11\n" \
  "v_pk_fma_f32 v[12:13], v[12:13], v[192:193], v[172:173]\n" \
  "v_cvt_pk_bf16_f32 v70, v12, v13\n" \
  "v_pk_fma_f32 v[14:15], v[14:15], v[194:195], v[174:175]\n" \
  "v_cvt_pk_bf16_f32 v71, v14, v15\n" \
  "buffer_store_dwordx4 v[68:71], v145, s[28:31], 0 offen\n" \
  "v_pk_fma_f32 v[0:1], v[0:1], v[180:181], v[160:161]\n" \
  "v_cvt_pk_bf16_f32 v64, v0, v1\n" \
  "v_pk_fma_f32 v[2:3], v[2:3], v[182:183], v[162:163]\n" \
  "v_cvt_pk_bf16_f32 v65, v2, v3\n" \
  "v_pk_fma_f32 v[4:5], v[4:5], v[184:185], v[164:165]\n" \
  "v_cvt_pk_bf16_f32 v66, v4, v5\n" \
  "v_pk_fma_f32 v[6:7], v[6:7], v[186:187], v[166:167]\n" \
  "v_cvt_pk_bf16_f32 v67, v6, v7\n" \
  "buffer_store_dwordx4 v[64:67], v144, s[28:31], 0 offen\n" \
  "s_waitcnt vmcnt(2)\n" \
  "v_xor_b32 v52, s71, v21\n" \
  "v_bitop3_b32 v52, v23, v52, s71 bitop3:0xde\n" \
  "v_bitop3_b32 v52, v25, v52, s71 bitop3:0xde\n" \
  "v_bitop3_b32 v52, v27, v52, s71 bitop3:0xde\n" \
  "v_bitop3_b32 v52, v29, v52, s71 bitop3:0xde\n" \
  "v_bitop3_b32 v52, v31, v52, s71 bitop3:0xde\n" \
  "v_cmp_ne_u32 vcc, 0, v52\n" \
  "s_cbranch_vccnz SHS_%=\n" \
  "SHK_%=:\n" \
  "v_mfma_f32_16x16x32_bf16 v[16:19], v[156:159], v[20:23], v[16:19]\n" \
  "v_mfma_f32_16x16x32_bf16 v[16:19], v[156:159], v[24:27], v[16:19]\n" \
  "v_mfma_f32_16x16x32_bf16 v[16:19], v[156:159], v[28:31], v[16:19]\n" \
  "s_or_b32 s60, %[bid], s68\n" \
  "s_cselect_b32 s60, 33554432, 0\n" \
  "s_mov_b64 exec, 1\n" \
  "buffer_store_dword v135, off, s[40:43], s60 sc1\n" \
  "s_mov_b64 exec, -1\n" \
  "s_nop 2\n" \
  "v_pk_fma_f32 v[16:17], v[16:17], v[196:197], v[176:177]\n" \
  "v_cvt_pk_bf16_f32 v72, v16, v17\n" \
  "v_pk_fma_f32 v[18:19], v[18:19], v[198:199], v[178:179]\n" \
  "v_cvt_pk_bf16_f32 v73, v18, v19\n" \
  "buffer_store_dwordx2 v[72:73], v146, s[28:31], 0 offen\n" \
  "s_endpgm\n" \
  "F2S_%=:\n" \
  "s_mov_b32 s74, 0\n" \
  "F2L_%=:\n" \
  "s_add_u32 s74, s74, 1\n" \
  "buffer_load_dword v52, v134, s[40:43], 0 offen sc1\n" \
  "s_waitcnt vmcnt(0)\n" \
  "v_cmp_ne_u32 vcc, v49, v52\n" \
  "s_cbranch_vccz F2K_%=\n" \
  "s_cmp_lt_u32 s74, 32768\n" \
  "s_cbranch_scc1 F2L_%=\n" \
  "s_branch F2K_%=\n" \
  "I0S_%=:\n" \
  "s_mov_b32 s74, 0\n" \
  "I0L_%=:\n" \
  "s_add_u32 s74, s74, 1\n" \
  "buffer_load_dword v246, v155, s[40:43], s84 offen offset:3072 sc1\n" \
  "s_waitcnt vmcnt(0)\n" \
  "v_cmp_eq_u32 vcc, s71, v246\n" \
  "s_cbranch_vccnz I0K_%=\n" \
  "s_cmp_lt_u32 s74, 32768\n" \
  "s_cbranch_scc1 I0L_%=\n" \
  "s_branch I0K_%=\n" \
  "I1S_%=:\n" \
  "s_mov_b32 s74, 0\n" \
  "I1L_%=:\n" \
  "s_add_u32 s74, s74, 1\n" \
  "buffer_load_dword v247, v155, s[40:43], s85 offen offset:3072 sc1\n" \
  "s_waitcnt vmcnt(0)\n" \
  "v_cmp_eq_u32 vcc, s71, v247\n" \
  "s_cbranch_vccnz I1K_%=\n" \
  "s_cmp_lt_u32 s74, 32768\n" \
  "s_cbranch_scc1 I1L_%=\n" \
  "s_branch I1K_%=\n" \
  "I2S_%=:\n" \
  "s_mov_b32 s74, 0\n" \
  "I2L_%=:\n" \
  "s_add_u32 s74, s74, 1\n" \
  "buffer_load_dword v246, v155, s[40:43], s86 offen offset:3072 sc1\n" \
  "s_waitcnt vmcnt(0)\n" \
  "v_cmp_eq_u32 vcc, s71, v246\n" \
  "s_cbranch_vccnz I2K_%=\n" \
  "s_cmp_lt_u32 s74, 32768\n" \
  "s_cbranch_scc1 I2L_%=\n" \
  "s_branch I2K_%=\n" \
  "STS_%=:\n" \
  "s_mov_b32 s74, 0\n" \
  "STL_%=:\n" \
  "s_add_u32 s74, s74, 1\n" \
  "v_lshlrev_b32 v58, 3, v147\n" \
  "v_add_u32 v58, 16384, v58\n" \
  "v_xor_b32 v59, 128, v58\n" \
  "buffer_load_dwordx2 v[242:243], v58, s[40:43], 0 offen sc1\n" \
  "buffer_load_dwordx2 v[244:245], v59, s[40:43], 0 offen sc1\n" \
  "buffer_load_dwordx4 v[200:203], v155, s[40:43], 0 offen offset:1024 sc1\n" \
  "s_waitcnt vmcnt(0)\n" \
  "v_xor_b32 v48, s71, v201\n" \
  "v_bitop3_b32 v48, v203, v48, s71 bitop3:0xde\n" \
  "v_cndmask_b32 v48, 0, v48, s[72:73]\n" \
  "v_bitop3_b32 v48, v243, v48, s71 bitop3:0xde\n" \
  "v_bitop3_b32 v48, v245, v48, s71 bitop3:0xde\n" \
  "v_cmp_ne_u32 vcc, 0, v48\n" \
  "s_cbranch_vccz STK_%=\n" \
  "s_cmp_lt_u32 s74, 32768\n" \
  "s_cbranch_scc1 STL_%=\n" \
  "s_branch STK_%=\n" \
  "RCS_%=:\n" \
  "s_mov_b32 s74, 0\n" \
  "RCL_%=:\n" \
  "s_add_u32 s74, s74, 1\n" \
  "buffer_load_dwordx4 v[204:207], v152, s[40:43], 0 offen sc1\n" \
  "s_waitcnt vmcnt(0)\n" \
  "v_xor_b32 v208, s71, v205\n" \
  "v_bitop3_b32 v208, v207, v208, s71 bitop3:0xde\n" \
  "v_and_b32 v208, v208, v154\n" \
  "v_cmp_ne_u32 vcc, 0, v208\n" \
  "s_cbranch_vccz RCK_%=\n" \
  "s_cmp_lt_u32 s74, 32768\n" \
  "s_cbranch_scc1 RCL_%=\n" \
  "s_branch RCK_%=\n" \
  "GTS_%=:\n" \
  "s_mov_b32 s74, 0\n" \
  "GTL_%=:\n" \
  "s_add_u32 s74, s74, 1\n" \
  "buffer_load_dwordx4 v[32:35], v149, s[40:43], 0 offen offset:0 sc1\n" \
  "buffer_load_dwordx4 v[36:39], v149, s[40:43], 0 offen offset:16 sc1\n" \
  "buffer_load_dwordx4 v[40:43], v150, s[40:43], 0 offen offset:0 sc1\n" \
  "buffer_load_dwordx4 v[44:47], v150, s[40:43], 0 offen offset:16 sc1\n" \
  "PAD6_%=:\n" \
  "s_cbranch_execz WRET_%=\n" \
  "buffer_load_dwordx4 v[48:51], v151, s[40:43], 0 offen offset:0 sc1\n" \
  "s_waitcnt vmcnt(0)\n" \
  "v_xor_b32 v52, s71, v33\n" \
  "v_bitop3_b32 v52, v35, v52, s71 bitop3:0xde\n" \
  "v_bitop3_b32 v52, v37, v52, s71 bitop3:0xde\n" \
  "v_bitop3_b32 v52, v39, v52, s71 bitop3:0xde\n" \
  "v_bitop3_b32 v52, v49, v52, s71 bitop3:0xde\n" \
  "v_bitop3_b32 v52, v51, v52, s71 bitop3:0xde\n" \
  "v_xor_b32 v53, s71, v41\n" \
  "v_bitop3_b32 v53, v43, v53, s71 bitop3:0xde\n" \
  "v_bitop3_b32 v53, v45, v53, s71 bitop3:0xde\n" \
  "v_bitop3_b32 v53, v47, v53, s71 bitop3:0xde\n" \
  "v_cndmask_b32 v52, 0, v52, s[92:93]\n" \
  "v_cndmask_b32 v53, 0, v53, s[94:95]\n" \
  "v_or_b32 v52, v52, v53\n" \
  "v_cmp_ne_u32 vcc, 0, v52\n" \
  "s_cbranch_vccz GTK_%=\n" \
  "s_cmp_lt_u32 s74, 32768\n" \
  "s_cbranch_scc1 GTL_%=\n" \
  "s_branch GTK_%=\n" \
  "F1S_%=:\n" \
  "s_mov_b32 s74, 0\n" \
  "F1L_%=:\n" \
  "s_add_u32 s74, s74, 1\n" \
  "buffer_load_dword v52, v134, s[40:43], 0 offen sc1\n" \
  "s_waitcnt vmcnt(0)\n" \
  "v_cmp_ne_u32 vcc, v49, v52\n" \
  "s_cbranch_vccz F1K_%=\n" \
  "s_cmp_lt_u32 s74, 32768\n" \
  "s_cbranch_scc1 F1L_%=\n" \
  "s_branch F1K_%=\n" \
  "SHS_%=:\n" \
  "s_mov_b32 s74, 0\n" \
  "SHL_%=:\n" \
  "s_add_u32 s74, s74, 1\n" \
  "buffer_load_dwordx4 v[20:23], v128, s[40:43], s84 offen offset:2048 sc1\n" \
  "buffer_load_dwordx4 v[24:27], v128, s[40:43], s85 offen offset:2048 sc1\n" \
  "buffer_load_dwordx4 v[28:31], v128, s[40:43], s86 offen offset:2048 sc1\n" \
  "s_waitcnt vmcnt(0)\n" \
  "v_xor_b32 v52, s71, v21\n" \
  "v_bitop3_b32 v52, v23, v52, s71 bitop3:0xde\n" \
  "v_bitop3_b32 v52, v25, v52, s71 bitop3:0xde\n" \
  "v_bitop3_b32 v52, v27, v52, s71 bitop3:0xde\n" \
  "v_bitop3_b32 v52, v29, v52, s71 bitop3:0xde\n" \
  "v_bitop3_b32 v52, v31, v52, s71 bitop3:0xde\n" \
  "v_cmp_ne_u32 vcc, 0, v52\n" \
  "s_cbranch_vccz SHK_%=\n" \
  "s_cmp_lt_u32 s74, 32768\n" \
  "s_cbranch_scc1 SHL_%=\n" \
  "s_branch SHK_%=\n" \
  "IBEG_%=:\n" \
  "PAD7_%=:\n" \
  "s_cbranch_execz WRET_%=\n" \
  "s_getpc_b64 s[98:99]\n" \
  "s_branch WARM_%=\n" \
  "s_sleep 24\n" \
  "s_load_dwordx4 s[64:67], %[ka], 0x90\n" \
  "s_add_u32 m0, s68, 98304\n" \
  "s_nop 0\n" \
  "buffer_load_dwordx4 v136, s[48:51], s70 offen offset:0 lds\n" \
  "buffer_load_dwordx4 v137, s[48:51], s70 offen offset:1024 lds\n" \
  "buffer_load_dwordx4 v138, s[48:51], s70 offen offset:2048 lds\n" \
  "buffer_load_dwordx4 v139, s[48:51], s70 offen offset:3072 lds\n" \
  "s_add_u32 s70, s70, 128\n" \
  "s_and_b32 s70, s70, 2047\n" \
  "s_add_u32 m0, s68, 114688\n" \
  "s_nop 0\n" \
  "buffer_load_dwordx4 v136, s[48:51], s70 offen offset:0 lds\n" \
  "buffer_load_dwordx4 v137, s[48:51], s70 offen offset:1024 lds\n" \
  "buffer_load_dwordx4 v138, s[48:51], s70 offen offset:2048 lds\n" \
  "buffer_load_dwordx4 v139, s[48:51], s70 offen offset:3072 lds\n" \
  "s_add_u32 s70, s70, 128\n" \
  "s_and_b32 s70, s70, 2047\n" \
  "s_add_u32 m0, s68, 131072\n" \
  "s_nop 0\n" \
  "buffer_load_dwordx4 v136, s[48:51], s70 offen offset:0 lds\n" \
  "buffer_load_dwordx4 v137, s[48:51], s70 offen offset:1024 lds\n" \
  "buffer_load_dwordx4 v138, s[48:51], s70 offen offset:2048 lds\n" \
  "buffer_load_dwordx4 v139, s[48:51], s70 offen offset:3072 lds\n" \
  "s_add_u32 s70, s70, 128\n" \
  "s_and_b32 s70, s70, 2047\n" \
  "s_add_u32 m0, s68, 147456\n" \
  "s_nop 0\n" \
  "buffer_load_dwordx4 v136, s[48:51], s70 offen offset:0 lds\n" \
  "buffer_load_dwordx4 v137, s[48:51], s70 offen offset:1024 lds\n" \
  "buffer_load_dwordx4 v138, s[48:51], s70 offen offset:2048 lds\n" \
  "buffer_load_dwordx4 v139, s[48:51], s70 offen offset:3072 lds\n" \
  "s_add_u32 s70, s70, 128\n" \
  "s_and_b32 s70, s70, 2047\n" \
  "s_waitcnt vmcnt(12) lgkmcnt(0)\n" \
  "ds_read_b128 a[0:3], v140 offset:0\n" \
  "ds_read_b128 a[4:7], v141 offset:0\n" \
  "ds_read_b128 a[8:11], v142 offset:0\n" \
  "ds_read_b128 a[12:15], v143 offset:0\n" \
  "s_waitcnt vmcnt(8) lgkmcnt(0)\n" \
  "ds_read_b128 a[16:19], v140 offset:16384\n" \
  "ds_read_b128 a[20:23], v141 offset:16384\n" \
  "ds_read_b128 a[24:27], v142 offset:16384\n" \
  "ds_read_b128 a[28:31], v143 offset:16384\n" \
  "s_add_u32 m0, s68, 98304\n" \
  "s_nop 0\n" \
  "buffer_load_dwordx4 v136, s[48:51], s70 offen offset:0 lds\n" \
  "buffer_load_dwordx4 v137, s[48:51], s70 offen offset:1024 lds\n" \
  "buffer_load_dwordx4 v138, s[48:51], s70 offen offset:2048 lds\n" \
  "buffer_load_dwordx4 v139, s[48:51], s70 offen offset:3072 lds\n" \
  "s_add_u32 s70, s70, 128\n" \
  "s_and_b32 s70, s70, 2047\n" \
  "s_lshr_b32 s61, %[bid], 6\n" \
  "s_lshl_b32 s61, s61, 3\n" \
  "s_and_b32 s62, %[bid], 7\n" \
  "s_add_u32 s61, s61, s62\n" \
  "s_lshl_b32 s61, s61, 1\n" \
  "s_bfe_u32 s62, %[bid], 0x10005\n" \
  "s_add_u32 s61, s61, s62\n" \
  "s_lshl_b32 s61, s61, 5\n" \
  "v_lshrrev_b32 v144, 3, %[tid]\n" \
  "v_add_u32 v144, s61, v144\n" \
  "v_mul_u32_u24 v144, 1280, v144\n" \
  "v_and_b32 v145, 7, %[tid]\n" \
  "v_lshl_add_u32 v144, v145, 7, v144\n" \
  "buffer_load_dword v248, v144, s[64:67], 0 offen offset:256\n" \
  "s_waitcnt vmcnt(9) lgkmcnt(0)\n" \
  "ds_read_b128 a[32:35], v140 offset:32768\n" \
  "ds_read_b128 a[36:39], v141 offset:32768\n" \
  "ds_read_b128 a[40:43], v142 offset:32768\n" \
  "ds_read_b128 a[44:47], v143 offset:32768\n" \
  "s_add_u32 m0, s68, 114688\n" \
  "s_nop 0\n" \
  "buffer_load_dwordx4 v136, s[48:51], s70 offen offset:0 lds\n" \
  "buffer_load_dwordx4 v137, s[48:51], s70 offen offset:1024 lds\n" \
  "buffer_load_dwordx4 v138, s[48:51], s70 offen offset:2048 lds\n" \
  "buffer_load_dwordx4 v139, s[48:51], s70 offen offset:3072 lds\n" \
  "s_add_u32 s70, s70, 128\n" \
  "s_and_b32 s70, s70, 2047\n" \
  "s_waitcnt vmcnt(9) lgkmcnt(0)\n" \
  "ds_read_b128 a[48:51], v140 offset:49152\n" \
  "ds_read_b128 a[52:55], v141 offset:49152\n" \
  "ds_read_b128 a[56:59], v142 offset:49152\n" \
  "ds_read_b128 a[60:63], v143 offset:49152\n" \
  "s_add_u32 m0, s68, 131072\n" \
  "s_nop 0\n" \
  "buffer_load_dwordx4 v136, s[48:51], s70 offen offset:0 lds\n" \
  "buffer_load_dwordx4 v137, s[48:51], s70 offen offset:1024 lds\n" \
  "buffer_load_dwordx4 v138, s[48:51], s70 offen offset:2048 lds\n" \
  "buffer_load_dwordx4 v139, s[48:51], s70 offen offset:3072 lds\n" \
  "s_add_u32 s70, s70, 128\n" \
  "s_and_b32 s70, s70, 2047\n" \
  "s_waitcnt vmcnt(9) lgkmcnt(0)\n" \
  "ds_read_b128 a[64:67], v140 offset:0\n" \
  "ds_read_b128 a[68:71], v141 offset:0\n" \
  "ds_read_b128 a[72:75], v142 offset:0\n" \
  "ds_read_b128 a[76:79], v143 offset:0\n" \
  "s_add_u32 m0, s68, 147456\n" \
  "s_nop 0\n" \
  "buffer_load_dwordx4 v136, s[48:51], s70 offen offset:0 lds\n" \
  "buffer_load_dwordx4 v137, s[48:51], s70 offen offset:1024 lds\n" \
  "buffer_load_dwordx4 v138, s[48:51], s70 offen offset:2048 lds\n" \
  "buffer_load_dwordx4 v139, s[48:51], s70 offen offset:3072 lds\n" \
  "s_add_u32 s70, s70, 128\n" \
  "s_and_b32 s70, s70, 2047\n" \
  "v_and_b32 v144, 31, %[tid]\n" \
  "v_bfe_u32 v145, %[tid], 3, 3\n" \
  "v_sub_u32 v144, v144, v145\n" \
  "v_lshl_add_u32 v144, v144, 12, v136\n" \
  "v_and_b32 v145, 32, %[tid]\n" \
  "v_lshl_add_u32 v144, v145, 2, v144\n" \
  "s_and_b32 s61, s70, 0x700\n" \
  "buffer_load_dword v248, v144, s[48:51], s61 offen\n" \
  "s_add_u32 s61, s61, 256\n" \
  "s_and_b32 s61, s61, 2047\n" \
  "buffer_load_dword v248, v144, s[48:51], s61 offen\n" \
  "s_add_u32 s61, s61, 256\n" \
  "s_and_b32 s61, s61, 2047\n" \
  "v_mov_b32 v247, 33554432\n" \
  "s_branch PBEG_%=\n" \
  "FBEG_%=:\n" \
  "PAD8_%=:\n" \
  "s_cbranch_execz WRET_%=\n" \
  "s_load_dwordx4 s[56:59], %[ka], 0x70\n" \
  "s_load_dwordx4 s[64:67], %[ka], 0x90\n" \
  "s_lshr_b32 s79, s68, 12\n" \
  "s_mul_i32 s81, s79, 20480\n" \
  "s_add_u32 m0, s81, 8192\n" \
  "s_mov_b32 s60, 221184\n" \
  "buffer_load_dwordx4 v128, s[52:55], s60 offen offset:0 nt lds\n" \
  "buffer_load_dwordx4 v129, s[52:55], s60 offen offset:1024 nt lds\n" \
  "buffer_load_dwordx4 v130, s[52:55], s60 offen offset:2048 nt lds\n" \
  "buffer_load_dwordx4 v131, s[52:55], s60 offen offset:3072 nt lds\n" \
  "s_lshr_b32 s80, %[bid], 6\n" \
  "s_lshl_b32 s80, s80, 3\n" \
  "s_and_b32 s61, %[bid], 7\n" \
  "s_add_u32 s80, s80, s61\n" \
  "v_and_b32 v0, 63, %[tid]\n" \
  "v_and_b32 v1, 15, %[tid]\n" \
  "v_bfe_u32 v2, %[tid], 4, 2\n" \
  "v_bfe_u32 v26, %[tid], 3, 2\n" \
  "v_bfe_u32 v27, %[tid], 2, 2\n" \
  "v_and_b32 v28, 3, %[tid]\n" \
  "v_lshlrev_b32 v28, 3, v28\n" \
  "v_lshl_or_b32 v28, v27, 7, v28\n" \
  "v_lshl_or_b32 v27, v26, 5, v28\n" \
  "v_lshl_or_b32 v120, v2, 10, v27\n" \
  "v_add_u32 v120, s81, v120\n" \
  "v_xor_b32 v121, 32, v120\n" \
  "v_xor_b32 v122, 64, v120\n" \
  "v_xor_b32 v123, 96, v120\n" \
  "s_lshl_b32 s61, s79, 5\n" \
  "v_xor_b32 v39, s61, v27\n" \
  "v_and_b32 v26, 1, v2\n" \
  "v_lshl_or_b32 v39, v26, 10, v39\n" \
  "v_lshrrev_b32 v27, 1, v2\n" \
  "s_movk_i32 s61, 20480\n" \
  "v_mad_u32_u24 v39, v27, s61, v39\n" \
  "s_mul_i32 s61, s80, 10240\n" \
  "s_lshl_b32 s62, s79, 7\n" \
  "s_add_u32 s61, s61, s62\n" \
  "s_add_u32 s61, s61, 131072\n" \
  "v_lshl_add_u32 v40, v1, 3, s61\n" \
  "v_lshl_add_u32 v40, v27, 9, v40\n" \
  "s_movk_i32 s61, 5120\n" \
  "v_mad_u32_u24 v40, v26, s61, v40\n" \
  "s_add_u32 m0, s81, 12288\n" \
  "s_mov_b32 s60, 331776\n" \
  "buffer_load_dwordx4 v128, s[52:55], s60 offen offset:0 nt lds\n" \
  "buffer_load_dwordx4 v129, s[52:55], s60 offen offset:1024 nt lds\n" \
  "buffer_load_dwordx4 v130, s[52:55], s60 offen offset:2048 nt lds\n" \
  "buffer_load_dwordx4 v131, s[52:55], s60 offen offset:3072 nt lds\n" \
  "s_lshl_b32 s61, s79, 11\n" \
  "s_add_u32 s61, s61, 8192\n" \
  "v_lshl_add_u32 v3, v0, 4, s61\n" \
  "s_lshl_b32 s61, s79, 9\n" \
  "s_sub_u32 s61, 2047, s61\n" \
  "v_lshlrev_b32 v26, 2, v0\n" \
  "v_sub_u32 v8, s61, v26\n" \
  "v_subrev_u32 v9, 1, v8\n" \
  "v_subrev_u32 v10, 2, v8\n" \
  "v_subrev_u32 v11, 3, v8\n" \
  "v_subrev_u32 v12, 256, v8\n" \
  "v_subrev_u32 v13, 257, v8\n" \
  "v_subrev_u32 v14, 258, v8\n" \
  "v_subrev_u32 v15, 259, v8\n" \
  "s_lshl_b32 s61, s79, 5\n" \
  "s_add_u32 s61, s61, 86016\n" \
  "v_lshl_add_u32 v4, v2, 3, s61\n" \
  "v_lshlrev_b32 v5, 3, v1\n" \
  "s_lshl_b32 s62, s79, 10\n" \
  "s_add_u32 s61, s62, 81920\n" \
  "v_min_u32 v6, 39, v0\n" \
  "v_lshl_add_u32 v33, v6, 4, s61\n" \
  "v_and_b32 v26, 1, %[tid]\n" \
  "v_mul_u32_u24 v26, 320, v26\n" \
  "v_lshl_add_u32 v26, v2, 4, v26\n" \
  "v_add_u32 v32, s61, v26\n" \
  "s_add_u32 s61, s62, 86272\n" \
  "v_lshl_add_u32 v34, v1, 3, s61\n" \
  "v_lshlrev_b32 v35, 3, v0\n" \
  "v_add_u32 v35, 86272, v35\n" \
  "s_lshl_b32 s62, s79, 8\n" \
  "s_add_u32 s61, s62, 90368\n" \
  "v_lshl_add_u32 v36, v0, 1, s61\n" \
  "v_bfe_u32 v26, %[tid], 2, 1\n" \
  "v_lshlrev_b32 v26, 7, v26\n" \
  "v_lshl_add_u32 v26, v2, 4, v26\n" \
  "v_add_u32 v26, s61, v26\n" \
  "s_add_u32 s61, s62, 91392\n" \
  "v_mov_b32 v27, s61\n" \
  "v_cmp_gt_u32 vcc, 8, v1\n" \
  "v_lshl_add_u32 v28, v0, 2, s61\n" \
  "v_mov_b32 v29, 0\n" \
  "v_cndmask_b32 v37, v27, v26, vcc\n" \
  "v_cndmask_b32 v38, v26, v27, vcc\n" \
  "ds_write_b32 v28, v29\n" \
  "v_cmp_le_u32 s[84:85], 20, v6\n" \
  "s_mul_i32 s61, s79, 20\n" \
  "v_add_u32 v26, s61, v6\n" \
  "v_subrev_u32 v27, 20, v26\n" \
  "v_cndmask_b32 v26, v26, v27, s[84:85]\n" \
  "v_lshlrev_b32 v6, 4, v26\n" \
  "s_waitcnt lgkmcnt(0)\n" \
  "s_getpc_b64 s[98:99]\n" \
  "s_branch WARM_%=\n" \
  "s_add_u32 m0, s81, 16384\n" \
  "s_mov_b32 s60, 442368\n" \
  "buffer_load_dwordx4 v128, s[52:55], s60 offen offset:0 nt lds\n" \
  "buffer_load_dwordx4 v129, s[52:55], s60 offen offset:1024 nt lds\n" \
  "buffer_load_dwordx4 v130, s[52:55], s60 offen offset:2048 nt lds\n" \
  "buffer_load_dwordx4 v131, s[52:55], s60 offen offset:3072 nt lds\n" \
  "s_waitcnt vmcnt(16)\n" \
  "ds_read_b64_tr_b16 a[96:97], v120 offset:0\n" \
  "ds_read_b64_tr_b16 a[98:99], v120 offset:512\n" \
  "ds_read_b64_tr_b16 a[100:101], v121 offset:0\n" \
  "ds_read_b64_tr_b16 a[102:103], v121 offset:512\n" \
  "ds_read_b64_tr_b16 a[104:105], v122 offset:0\n" \
  "ds_read_b64_tr_b16 a[106:107], v122 offset:512\n" \
  "ds_read_b64_tr_b16 a[108:109], v123 offset:0\n" \
  "ds_read_b64_tr_b16 a[110:111], v123 offset:512\n" \
  "s_waitcnt lgkmcnt(0)\n" \
  "s_add_u32 m0, s81, 0\n" \
  "s_mov_b32 s60, 0\n" \
  "buffer_load_dwordx4 v128, s[56:59], s60 offen offset:0 nt lds\n" \
  "buffer_load_dwordx4 v129, s[56:59], s60 offen offset:1024 nt lds\n" \
  "buffer_load_dwordx4 v130, s[56:59], s60 offen offset:2048 nt lds\n" \
  "buffer_load_dwordx4 v131, s[56:59], s60 offen offset:3072 nt lds\n" \
  "s_barrier\n" \
  "buffer_load_dwordx4 v[16:19], v3, s[40:43], 0 offen offset:0 sc1\n" \
  "buffer_load_dwordx4 v[20:23], v3, s[40:43], 0 offen offset:1024 sc1\n" \
  "s_waitcnt vmcnt(18)\n" \
  "ds_read_b64_tr_b16 a[112:113], v120 offset:4096\n" \
  "ds_read_b64_tr_b16 a[114:115], v120 offset:4608\n" \
  "ds_read_b64_tr_b16 a[116:117], v121 offset:4096\n" \
  "ds_read_b64_tr_b16 a[118:119], v121 offset:4608\n" \
  "ds_read_b64_tr_b16 a[120:121], v122 offset:4096\n" \
  "ds_read_b64_tr_b16 a[122:123], v122 offset:4608\n" \
  "ds_read_b64_tr_b16 a[124:125], v123 offset:4096\n" \
  "ds_read_b64_tr_b16 a[126:127], v123 offset:4608\n" \
  "s_waitcnt lgkmcnt(0)\n" \
  "s_add_u32 m0, s81, 4096\n" \
  "s_mov_b32 s60, 110592\n" \
  "buffer_load_dwordx4 v128, s[56:59], s60 offen offset:0 nt lds\n" \
  "buffer_load_dwordx4 v129, s[56:59], s60 offen offset:1024 nt lds\n" \
  "buffer_load_dwordx4 v130, s[56:59], s60 offen offset:2048 nt lds\n" \
  "buffer_load_dwordx4 v131, s[56:59], s60 offen offset:3072 nt lds\n" \
  "s_waitcnt vmcnt(18)\n" \
  "ds_read_b64_tr_b16 a[128:129], v120 offset:8192\n" \
  "ds_read_b64_tr_b16 a[130:131], v120 offset:8704\n" \
  "ds_read_b64_tr_b16 a[132:133], v121 offset:8192\n" \
  "ds_read_b64_tr_b16 a[134:135], v121 offset:8704\n" \
  "ds_read_b64_tr_b16 a[136:137], v122 offset:8192\n" \
  "ds_read_b64_tr_b16 a[138:139], v122 offset:8704\n" \
  "ds_read_b64_tr_b16 a[140:141], v123 offset:8192\n" \
  "ds_read_b64_tr_b16 a[142:143], v123 offset:8704\n" \
  "s_waitcnt lgkmcnt(0)\n" \
  "s_add_u32 m0, s81, 8192\n" \
  "s_mov_b32 s60, 221184\n" \
  "buffer_load_dwordx4 v128, s[56:59], s60 offen offset:0 nt lds\n" \
  "buffer_load_dwordx4 v129, s[56:59], s60 offen offset:1024 nt lds\n" \
  "buffer_load_dwordx4 v130, s[56:59], s60 offen offset:2048 nt lds\n" \
  "buffer_load_dwordx4 v131, s[56:59], s60 offen offset:3072 nt lds\n" \
  "s_waitcnt vmcnt(8)\n" \
  "v_xor_b32 v26, s71, v17\n" \
  "v_bitop3_b32 v26, v19, v26, s71 bitop3:0xde\n" \
  "v_bitop3_b32 v26, v21, v26, s71 bitop3:0xde\n" \
  "v_bitop3_b32 v26, v23, v26, s71 bitop3:0xde\n" \
  "v_cmp_ne_u32 vcc, 0, v26\n" \
  "s_cbranch_vccnz RGS_%=\n" \
  "RGK_%=:\n" \
  "s_mov_b32 s61, 0x80008000\n" \
  "s_mov_b32 s62, 0xffff0000\n" \
  "v_pk_ashrrev_i16 v26, 15, v16 op_sel_hi:[0,1]\n" \
  "v_bitop3_b32 v16, v16, v26, s61 bitop3:0x1e\n" \
  "v_lshl_or_b32 v8, v16, 16, v8\n" \
  "v_and_or_b32 v9, v16, s62, v9\n" \
  "v_pk_ashrrev_i16 v26, 15, v18 op_sel_hi:[0,1]\n" \
  "v_bitop3_b32 v18, v18, v26, s61 bitop3:0x1e\n" \
  "v_lshl_or_b32 v10, v18, 16, v10\n" \
  "v_and_or_b32 v11, v18, s62, v11\n" \
  "v_pk_ashrrev_i16 v26, 15, v20 op_sel_hi:[0,1]\n" \
  "v_bitop3_b32 v20, v20, v26, s61 bitop3:0x1e\n" \
  "v_lshl_or_b32 v12, v20, 16, v12\n" \
  "v_and_or_b32 v13, v20, s62, v13\n" \
  "v_pk_ashrrev_i16 v26, 15, v22 op_sel_hi:[0,1]\n" \
  "v_bitop3_b32 v22, v22, v26, s61 bitop3:0x1e\n" \
  "v_lshl_or_b32 v14, v22, 16, v14\n" \
  "v_and_or_b32 v15, v22, s62, v15\n" \
  "v_max_u32 v24, v8, v9\n" \
  "v_min_u32 v25, v8, v9\n" \
  "v_med3_u32 v25, v24, v25, v10\n" \
  "v_max_u32 v24, v24, v10\n" \
  "v_med3_u32 v25, v24, v25, v11\n" \
  "v_max_u32 v24, v24, v11\n" \
  "v_med3_u32 v25, v24, v25, v12\n" \
  "v_max_u32 v24, v24, v12\n" \
  "v_med3_u32 v25, v24, v25, v13\n" \
  "v_max_u32 v24, v24, v13\n" \
  "v_med3_u32 v25, v24, v25, v14\n" \
  "v_max_u32 v24, v24, v14\n" \
  "v_med3_u32 v25, v24, v25, v15\n" \
  "v_max_u32 v24, v24, v15\n" \
  "s_nop 0\n" \
  "s_nop 0\n" \
  "v_min_u32_dpp v26, v24, v24 quad_perm:[1,0,3,2] row_mask:0xf bank_mask:0xf bound_ctrl:1\n" \
  "v_max_u32_dpp v25, v25, v25 quad_perm:[1,0,3,2] row_mask:0xf bank_mask:0xf bound_ctrl:1\n" \
  "v_max_u32_dpp v24, v24, v24 quad_perm:[1,0,3,2] row_mask:0xf bank_mask:0xf bound_ctrl:1\n" \
  "v_max_u32 v25, v25, v26\n" \
  "s_nop 0\n" \
  "v_min_u32_dpp v26, v24, v24 quad_perm:[2,3,0,1] row_mask:0xf bank_mask:0xf bound_ctrl:1\n" \
  "v_max_u32_dpp v25, v25, v25 quad_perm:[2,3,0,1] row_mask:0xf bank_mask:0xf bound_ctrl:1\n" \
  "v_max_u32_dpp v24, v24, v24 quad_perm:[2,3,0,1] row_mask:0xf bank_mask:0xf bound_ctrl:1\n" \
  "v_max_u32 v25, v25, v26\n" \
  "s_nop 0\n" \
  "v_min_u32_dpp v26, v24, v24 row_half_mirror row_mask:0xf bank_mask:0xf bound_ctrl:1\n" \
  "v_max_u32_dpp v25, v25, v25 row_half_mirror row_mask:0xf bank_mask:0xf bound_ctrl:1\n" \
  "v_max_u32_dpp v24, v24, v24 row_half_mirror row_mask:0xf bank_mask:0xf bound_ctrl:1\n" \
  "v_max_u32 v25, v25, v26\n" \
  "s_nop 0\n" \
  "v_min_u32_dpp v26, v24, v24 row_mirror row_mask:0xf bank_mask:0xf bound_ctrl:1\n" \
  "v_max_u32_dpp v25, v25, v25 row_mirror row_mask:0xf bank_mask:0xf bound_ctrl:1\n" \
  "v_max_u32_dpp v24, v24, v24 row_mirror row_mask:0xf bank_mask:0xf bound_ctrl:1\n" \
  "v_max_u32 v25, v25, v26\n" \
  "ds_write_b64 v4, v[24:25]\n" \
  "s_nop 0\n" \
  "ds_read_b64_tr_b16 a[144:145], v120 offset:12288\n" \
  "ds_read_b64_tr_b16 a[146:147], v120 offset:12800\n" \
  "ds_read_b64_tr_b16 a[148:149], v121 offset:12288\n" \
  "ds_read_b64_tr_b16 a[150:151], v121 offset:12800\n" \
  "ds_read_b64_tr_b16 a[152:153], v122 offset:12288\n" \
  "ds_read_b64_tr_b16 a[154:155], v122 offset:12800\n" \
  "ds_read_b64_tr_b16 a[156:157], v123 offset:12288\n" \
  "ds_read_b64_tr_b16 a[158:159], v123 offset:12800\n" \
  "s_waitcnt lgkmcnt(0)\n" \
  "s_add_u32 m0, s81, 12288\n" \
  "s_mov_b32 s60, 331776\n" \
  "buffer_load_dwordx4 v128, s[56:59], s60 offen offset:0 nt lds\n" \
  "buffer_load_dwordx4 v129, s[56:59], s60 offen offset:1024 nt lds\n" \
  "buffer_load_dwordx4 v130, s[56:59], s60 offen offset:2048 nt lds\n" \
  "buffer_load_dwordx4 v131, s[56:59], s60 offen offset:3072 nt lds\n" \
  "s_waitcnt lgkmcnt(0)\n" \
  "s_barrier\n" \
  "v_add_u32 v5, 86016, v5\n" \
  "ds_read_b64 v[24:25], v5\n" \
  "s_waitcnt lgkmcnt(0)\n" \
  "v_min_u32_dpp v26, v24, v24 quad_perm:[1,0,3,2] row_mask:0xf bank_mask:0xf bound_ctrl:1\n" \
  "v_max_u32_dpp v25, v25, v25 quad_perm:[1,0,3,2] row_mask:0xf bank_mask:0xf bound_ctrl:1\n" \
  "v_max_u32_dpp v24, v24, v24 quad_perm:[1,0,3,2] row_mask:0xf bank_mask:0xf bound_ctrl:1\n" \
  "v_max_u32 v25, v25, v26\n" \
  "s_nop 0\n" \
  "v_min_u32_dpp v26, v24, v24 quad_perm:[2,3,0,1] row_mask:0xf bank_mask:0xf bound_ctrl:1\n" \
  "v_max_u32_dpp v25, v25, v25 quad_perm:[2,3,0,1] row_mask:0xf bank_mask:0xf bound_ctrl:1\n" \
  "v_max_u32_dpp v24, v24, v24 quad_perm:[2,3,0,1] row_mask:0xf bank_mask:0xf bound_ctrl:1\n" \
  "v_max_u32 v25, v25, v26\n" \
  "s_nop 0\n" \
  "v_min_u32_dpp v26, v24, v24 row_half_mirror row_mask:0xf bank_mask:0xf bound_ctrl:1\n" \
  "v_max_u32_dpp v25, v25, v25 row_half_mirror row_mask:0xf bank_mask:0xf bound_ctrl:1\n" \
  "v_max_u32_dpp v24, v24, v24 row_half_mirror row_mask:0xf bank_mask:0xf bound_ctrl:1\n" \
  "v_max_u32 v25, v25, v26\n" \
  "s_nop 0\n" \
  "v_min_u32_dpp v26, v24, v24 row_mirror row_mask:0xf bank_mask:0xf bound_ctrl:1\n" \
  "v_max_u32_dpp v25, v25, v25 row_mirror row_mask:0xf bank_mask:0xf bound_ctrl:1\n" \
  "v_max_u32_dpp v24, v24, v24 row_mirror row_mask:0xf bank_mask:0xf bound_ctrl:1\n" \
  "v_max_u32 v25, v25, v26\n" \
  "v_cndmask_b32 v26, v24, v25, s[84:85]\n" \
  "s_movk_i32 s61, 2047\n" \
  "v_bfi_b32 v26, v26, 0, s61\n" \
  "s_movk_i32 s62, 1280\n" \
  "v_mad_u32_u24 v41, v26, s62, v6\n" \
  "v_lshlrev_b32 v42, 3, v26\n" \
  "v_add_u32 v42, 16384, v42\n" \
  "buffer_load_dwordx4 v[44:47], v41, s[36:39], 0 offen\n" \
  "buffer_load_dwordx2 v[48:49], v42, s[40:43], 0 offen sc1\n" \
  "s_lshl_b32 s62, s79, 8\n" \
  "s_or_b32 s62, s62, s80\n" \
  "s_cmp_lg_u32 s62, 16\n" \
  "s_cbranch_scc1 NOREC_%=\n" \
  "v_bfi_b32 v26, v24, 0, s61\n" \
  "v_bfi_b32 v27, v25, 0, s61\n" \
  "v_lshl_or_b32 v26, v27, 16, v26\n" \
  "s_mov_b32 s62, 0xffff0000\n" \
  "v_lshrrev_b32 v27, 16, v24\n" \
  "v_and_or_b32 v28, v25, s62, v27\n" \
  "v_not_b32 v27, v28\n" \
  "v_pk_ashrrev_i16 v27, 15, v27 op_sel_hi:[0,1]\n" \
  "s_mov_b32 s62, 0x80008000\n" \
  "v_bitop3_b32 v28, v28, v27, s62 bitop3:0x1e\n" \
  "v_mov_b32 v27, s71\n" \
  "v_mov_b32 v29, s71\n" \
  "v_lshlrev_b32 v30, 4, v0\n" \
  "s_mov_b64 exec, 0xff\n" \
  "buffer_store_dwordx4 v[26:29], v30, s[40:43], 0 offen offset:1024 sc1\n" \
  "s_mov_b64 exec, -1\n" \
  "s_nop 0\n" \
  "NOREC_%=:\n" \
  "s_nop 0\n" \
  "ds_read_b64_tr_b16 a[160:161], v120 offset:16384\n" \
  "ds_read_b64_tr_b16 a[162:163], v120 offset:16896\n" \
  "ds_read_b64_tr_b16 a[164:165], v121 offset:16384\n" \
  "ds_read_b64_tr_b16 a[166:167], v121 offset:16896\n" \
  "ds_read_b64_tr_b16 a[168:169], v122 offset:16384\n" \
  "ds_read_b64_tr_b16 a[170:171], v122 offset:16896\n" \
  "ds_read_b64_tr_b16 a[172:173], v123 offset:16384\n" \
  "ds_read_b64_tr_b16 a[174:175], v123 offset:16896\n" \
  "s_waitcnt lgkmcnt(0)\n" \
  "s_add_u32 m0, s81, 16384\n" \
  "s_mov_b32 s60, 442368\n" \
  "buffer_load_dwordx4 v128, s[56:59], s60 offen offset:0 nt lds\n" \
  "buffer_load_dwordx4 v129, s[56:59], s60 offen offset:1024 nt lds\n" \
  "buffer_load_dwordx4 v130, s[56:59], s60 offen offset:2048 nt lds\n" \
  "buffer_load_dwordx4 v131, s[56:59], s60 offen offset:3072 nt lds\n" \
  "s_nop 0\n" \
  "ds_read_b64_tr_b16 a[176:177], v120 offset:0\n" \
  "ds_read_b64_tr_b16 a[178:179], v120 offset:512\n" \
  "ds_read_b64_tr_b16 a[180:181], v121 offset:0\n" \
  "ds_read_b64_tr_b16 a[182:183], v121 offset:512\n" \
  "ds_read_b64_tr_b16 a[184:185], v122 offset:0\n" \
  "ds_read_b64_tr_b16 a[186:187], v122 offset:512\n" \
  "ds_read_b64_tr_b16 a[188:189], v123 offset:0\n" \
  "ds_read_b64_tr_b16 a[190:191], v123 offset:512\n" \
  "s_waitcnt lgkmcnt(0)\n" \
  "s_add_u32 m0, s81, 0\n" \
  "s_movk_i32 s60, 0\n" \
  "buffer_load_dwordx4 v132, s[64:67], s60 offen offset:0 nt lds\n" \
  "buffer_load_dwordx4 v133, s[64:67], s60 offen offset:1024 nt lds\n" \
  "s_add_u32 m0, s81, 2048\n" \
  "s_movk_i32 s60, 128\n" \
  "buffer_load_dwordx4 v132, s[64:67], s60 offen offset:0 nt lds\n" \
  "buffer_load_dwordx4 v133, s[64:67], s60 offen offset:1024 nt lds\n" \
  "s_waitcnt vmcnt(18)\n" \
  "ds_read_b64_tr_b16 a[192:193], v120 offset:4096\n" \
  "ds_read_b64_tr_b16 a[194:195], v120 offset:4608\n" \
  "ds_read_b64_tr_b16 a[196:197], v121 offset:4096\n" \
  "ds_read_b64_tr_b16 a[198:199], v121 offset:4608\n" \
  "ds_read_b64_tr_b16 a[200:201], v122 offset:4096\n" \
  "ds_read_b64_tr_b16 a[202:203], v122 offset:4608\n" \
  "ds_read_b64_tr_b16 a[204:205], v123 offset:4096\n" \
  "ds_read_b64_tr_b16 a[206:207], v123 offset:4608\n" \
  "s_waitcnt lgkmcnt(0)\n" \
  "s_add_u32 m0, s81, 4096\n" \
  "s_movk_i32 s60, 256\n" \
  "buffer_load_dwordx4 v132, s[64:67], s60 offen offset:0 nt lds\n" \
  "buffer_load_dwordx4 v133, s[64:67], s60 offen offset:1024 nt lds\n" \
  "s_add_u32 m0, s81, 6144\n" \
  "s_movk_i32 s60, 384\n" \
  "buffer_load_dwordx4 v132, s[64:67], s60 offen offset:0 nt lds\n" \
  "buffer_load_dwordx4 v133, s[64:67], s60 offen offset:1024 nt lds\n" \
  "s_waitcnt vmcnt(12)\n" \
  "v_cmp_ne_u32 vcc, s71, v49\n" \
  "s_cbranch_vccnz SGS_%=\n" \
  "SGK_%=:\n" \
  "v_and_b32 v50, 0xffff0000, v48\n" \
  "v_rcp_f32 v50, v50\n" \
  "v_lshlrev_b32 v48, 16, v48\n" \
  "s_nop 0\n" \
  "v_lshlrev_b32 v26, 16, v44\n" \
  "v_and_b32 v27, 0xffff0000, v44\n" \
  "ds_read_b64_tr_b16 a[208:209], v120 offset:8192\n" \
  "v_pk_add_f32 v[26:27], v[26:27], v[48:49] op_sel_hi:[1,0] neg_lo:[0,1] neg_hi:[0,1]\n" \
  "v_cvt_pk_bf16_f32 v27, v26, v27\n" \
  "ds_read_b64_tr_b16 a[210:211], v120 offset:8704\n" \
  "v_lshlrev_b32 v26, 16, v27\n" \
  "v_and_b32 v27, 0xffff0000, v27\n" \
  "v_pk_mul_f32 v[26:27], v[26:27], v[50:51] op_sel_hi:[1,0]\n" \
  "ds_read_b64_tr_b16 a[212:213], v121 offset:8192\n" \
  "v_cvt_pk_bf16_f32 v44, v26, v27\n" \
  "v_lshlrev_b32 v26, 16, v45\n" \
  "ds_read_b64_tr_b16 a[214:215], v121 offset:8704\n" \
  "v_and_b32 v27, 0xffff0000, v45\n" \
  "v_pk_add_f32 v[26:27], v[26:27], v[48:49] op_sel_hi:[1,0] neg_lo:[0,1] neg_hi:[0,1]\n" \
  "ds_read_b64_tr_b16 a[216:217], v122 offset:8192\n" \
  "v_cvt_pk_bf16_f32 v27, v26, v27\n" \
  "v_lshlrev_b32 v26, 16, v27\n" \
  "ds_read_b64_tr_b16 a[218:219], v122 offset:8704\n" \
  "v_and_b32 v27, 0xffff0000, v27\n" \
  "v_pk_mul_f32 v[26:27], v[26:27], v[50:51] op_sel_hi:[1,0]\n" \
  "v_cvt_pk_bf16_f32 v45, v26, v27\n" \
  "ds_read_b64_tr_b16 a[220:221], v123 offset:8192\n" \
  "v_lshlrev_b32 v26, 16, v46\n" \
  "v_and_b32 v27, 0xffff0000, v46\n" \
  "ds_read_b64_tr_b16 a[222:223], v123 offset:8704\n" \
  "v_pk_add_f32 v[26:27], v[26:27], v[48:49] op_sel_hi:[1,0] neg_lo:[0,1] neg_hi:[0,1]\n" \
  "v_cvt_pk_bf16_f32 v27, v26, v27\n" \
  "s_waitcnt lgkmcnt(0)\n" \
  "v_lshlrev_b32 v26, 16, v27\n" \
  "v_and_b32 v27, 0xffff0000, v27\n" \
  "v_pk_mul_f32 v[26:27], v[26:27], v[50:51] op_sel_hi:[1,0]\n" \
  "s_add_u32 m0, s81, 8192\n" \
  "v_cvt_pk_bf16_f32 v46, v26, v27\n" \
  "v_lshlrev_b32 v26, 16, v47\n" \
  "s_movk_i32 s60, 512\n" \
  "v_and_b32 v27, 0xffff0000, v47\n" \
  "v_pk_add_f32 v[26:27], v[26:27], v[48:49] op_sel_hi:[1,0] neg_lo:[0,1] neg_hi:[0,1]\n" \
  "buffer_load_dwordx4 v132, s[64:67], s60 offen offset:0 nt lds\n" \
  "v_cvt_pk_bf16_f32 v27, v26, v27\n" \
  "v_lshlrev_b32 v26, 16, v27\n" \
  "buffer_load_dwordx4 v133, s[64:67], s60 offen offset:1024 nt lds\n" \
  "v_and_b32 v27, 0xffff0000, v27\n" \
  "v_pk_mul_f32 v[26:27], v[26:27], v[50:51] op_sel_hi:[1,0]\n" \
  "v_cvt_pk_bf16_f32 v47, v26, v27\n" \
  "s_add_u32 m0, s81, 10240\n" \
  "ds_write_b128 v33, v[44:47]\n" \
  "ds_read_b128 v[100:103], v32 offset:0\n" \
  "s_movk_i32 s60, 640\n" \
  "ds_read_b128 v[104:107], v32 offset:64\n" \
  "ds_read_b128 v[108:111], v32 offset:128\n" \
  "buffer_load_dwordx4 v132, s[64:67], s60 offen offset:0 nt lds\n" \
  "ds_read_b128 v[112:115], v32 offset:192\n" \
  "ds_read_b128 v[116:119], v32 offset:256\n" \
  "buffer_load_dwordx4 v133, s[64:67], s60 offen offset:1024 nt lds\n" \
  "s_waitcnt lgkmcnt(0)\n" \
  "v_mfma_f32_16x16x32_bf16 v[64:67], v[100:103], a[96:99], 0\n" \
  "v_mfma_f32_16x16x32_bf16 v[68:71], v[100:103], a[100:103], 0\n" \
  "s_nop 0\n" \
  "v_mfma_f32_16x16x32_bf16 v[72:75], v[100:103], a[104:107], 0\n" \
  "ds_read_b64_tr_b16 a[224:225], v120 offset:12288\n" \
  "v_mfma_f32_16x16x32_bf16 v[76:79], v[100:103], a[108:111], 0\n" \
  "ds_read_b64_tr_b16 a[226:227], v120 offset:12800\n" \
  "v_mfma_f32_16x16x32_bf16 v[64:67], v[104:107], a[112:115], v[64:67]\n" \
  "v_mfma_f32_16x16x32_bf16 v[68:71], v[104:107], a[116:119], v[68:71]\n" \
  "ds_read_b64_tr_b16 a[228:229], v121 offset:12288\n" \
  "v_mfma_f32_16x16x32_bf16 v[72:75], v[104:107], a[120:123], v[72:75]\n" \
  "ds_read_b64_tr_b16 a[230:231], v121 offset:12800\n" \
  "v_mfma_f32_16x16x32_bf16 v[76:79], v[104:107], a[124:127], v[76:79]\n" \
  "ds_read_b64_tr_b16 a[232:233], v122 offset:12288\n" \
  "v_mfma_f32_16x16x32_bf16 v[64:67], v[108:111], a[128:131], v[64:67]\n" \
  "ds_read_b64_tr_b16 a[234:235], v122 offset:12800\n" \
  "v_mfma_f32_16x16x32_bf16 v[68:71], v[108:111], a[132:135], v[68:71]\n" \
  "v_mfma_f32_16x16x32_bf16 v[72:75], v[108:111], a[136:139], v[72:75]\n" \
  "ds_read_b64_tr_b16 a[236:237], v123 offset:12288\n" \
  "v_mfma_f32_16x16x32_bf16 v[76:79], v[108:111], a[140:143], v[76:79]\n" \
  "ds_read_b64_tr_b16 a[238:239], v123 offset:12800\n" \
  "v_mfma_f32_16x16x32_bf16 v[64:67], v[112:115], a[144:147], v[64:67]\n" \
  "s_waitcnt lgkmcnt(0)\n" \
  "v_mfma_f32_16x16x32_bf16 v[68:71], v[112:115], a[148:151], v[68:71]\n" \
  "v_mfma_f32_16x16x32_bf16 v[72:75], v[112:115], a[152:155], v[72:75]\n" \
  "s_add_u32 m0, s81, 12288\n" \
  "v_mfma_f32_16x16x32_bf16 v[76:79], v[112:115], a[156:159], v[76:79]\n" \
  "s_movk_i32 s60, 768\n" \
  "v_mfma_f32_16x16x32_bf16 v[64:67], v[116:119], a[160:163], v[64:67]\n" \
  "buffer_load_dwordx4 v132, s[64:67], s60 offen offset:0 nt lds\n" \
  "v_mfma_f32_16x16x32_bf16 v[68:71], v[116:119], a[164:167], v[68:71]\n" \
  "PAD9_%=:\n" \
  "s_cbranch_execz WRET_%=\n" \
  "buffer_load_dwordx4 v133, s[64:67], s60 offen offset:1024 nt lds\n" \
  "v_mfma_f32_16x16x32_bf16 v[72:75], v[116:119], a[168:171], v[72:75]\n" \
  "v_mfma_f32_16x16x32_bf16 v[76:79], v[116:119], a[172:175], v[76:79]\n" \
  "s_add_u32 m0, s81, 14336\n" \
  "v_mfma_f32_16x16x32_bf16 v[80:83], v[100:103], a[176:179], 0\n" \
  "s_movk_i32 s60, 896\n" \
  "v_mfma_f32_16x16x32_bf16 v[84:87], v[100:103], a[180:183], 0\n" \
  "buffer_load_dwordx4 v132, s[64:67], s60 offen offset:0 nt lds\n" \
  "v_mfma_f32_16x16x32_bf16 v[88:91], v[100:103], a[184:187], 0\n" \
  "v_mfma_f32_16x16x32_bf16 v[92:95], v[100:103], a[188:191], 0\n" \
  "buffer_load_dwordx4 v133, s[64:67], s60 offen offset:1024 nt lds\n" \
  "v_mfma_f32_16x16x32_bf16 v[80:83], v[104:107], a[192:195], v[80:83]\n" \
  "s_add_u32 m0, s68, 98304\n" \
  "v_mfma_f32_16x16x32_bf16 v[84:87], v[104:107], a[196:199], v[84:87]\n" \
  "buffer_load_dwordx4 v136, s[48:51], s70 offen offset:0 lds\n" \
  "v_mfma_f32_16x16x32_bf16 v[88:91], v[104:107], a[200:203], v[88:91]\n" \
  "buffer_load_dwordx4 v137, s[48:51], s70 offen offset:1024 lds\n" \
  "v_mfma_f32_16x16x32_bf16 v[92:95], v[104:107], a[204:207], v[92:95]\n" \
  "v_mfma_f32_16x16x32_bf16 v[80:83], v[108:111], a[208:211], v[80:83]\n" \
  "buffer_load_dwordx4 v138, s[48:51], s70 offen offset:2048 lds\n" \
  "v_mfma_f32_16x16x32_bf16 v[84:87], v[108:111], a[212:215], v[84:87]\n" \
  "buffer_load_dwordx4 v139, s[48:51], s70 offen offset:3072 lds\n" \
  "v_mfma_f32_16x16x32_bf16 v[88:91], v[108:111], a[216:219], v[88:91]\n" \
  "s_add_u32 s70, s70, 128\n" \
  "v_mfma_f32_16x16x32_bf16 v[92:95], v[108:111], a[220:223], v[92:95]\n" \
  "s_and_b32 s70, s70, 2047\n" \
  "v_mfma_f32_16x16x32_bf16 v[80:83], v[112:115], a[224:227], v[80:83]\n" \
  "s_waitcnt vmcnt(20)\n" \
  "ds_read_b64_tr_b16 a[240:241], v120 offset:16384\n" \
  "ds_read_b64_tr_b16 a[242:243], v120 offset:16896\n" \
  "ds_read_b64_tr_b16 a[244:245], v121 offset:16384\n" \
  "v_mfma_f32_16x16x32_bf16 v[84:87], v[112:115], a[228:231], v[84:87]\n" \
  "ds_read_b64_tr_b16 a[246:247], v121 offset:16896\n" \
  "ds_read_b64_tr_b16 a[248:249], v122 offset:16384\n" \
  "ds_read_b64_tr_b16 a[250:251], v122 offset:16896\n" \
  "ds_read_b64_tr_b16 a[252:253], v123 offset:16384\n" \
  "ds_read_b64_tr_b16 a[254:255], v123 offset:16896\n" \
  "v_mfma_f32_16x16x32_bf16 v[88:91], v[112:115], a[232:235], v[88:91]\n" \
  "s_waitcnt lgkmcnt(0)\n" \
  "s_add_u32 m0, s81, 16384\n" \
  "s_nop 0\n" \
  "s_movk_i32 s60, 1024\n" \
  "buffer_load_dwordx4 v132, s[64:67], s60 offen offset:0 nt lds\n" \
  "v_mfma_f32_16x16x32_bf16 v[92:95], v[112:115], a[236:239], v[92:95]\n" \
  "buffer_load_dwordx4 v133, s[64:67], s60 offen offset:1024 nt lds\n" \
  "s_add_u32 m0, s81, 18432\n" \
  "s_nop 0\n" \
  "s_movk_i32 s60, 1152\n" \
  "buffer_load_dwordx4 v132, s[64:67], s60 offen offset:0 nt lds\n" \
  "buffer_load_dwordx4 v133, s[64:67], s60 offen offset:1024 nt lds\n" \
  "v_mfma_f32_16x16x32_bf16 v[80:83], v[116:119], a[240:243], v[80:83]\n" \
  "v_mfma_f32_16x16x32_bf16 v[84:87], v[116:119], a[244:247], v[84:87]\n" \
  "v_mfma_f32_16x16x32_bf16 v[88:91], v[116:119], a[248:251], v[88:91]\n" \
  "v_mfma_f32_16x16x32_bf16 v[92:95], v[116:119], a[252:255], v[92:95]\n" \
  "s_nop 1\n" \
  "s_mov_b64 exec, 0xffff\n" \
  "ds_write_b64 v34, v[64:65] offset:0\n" \
  "ds_write_b64 v34, v[68:69] offset:128\n" \
  "ds_write_b64 v34, v[72:73] offset:256\n" \
  "ds_write_b64 v34, v[76:77] offset:384\n" \
  "ds_write_b64 v34, v[80:81] offset:512\n" \
  "ds_write_b64 v34, v[84:85] offset:640\n" \
  "ds_write_b64 v34, v[88:89] offset:768\n" \
  "ds_write_b64 v34, v[92:93] offset:896\n" \
  "s_mov_b64 exec, -1\n" \
  "s_mul_i32 s61, s79, 3\n" \
  "s_sub_u32 s61, s80, s61\n" \
  "s_cmp_ge_u32 s61, 3\n" \
  "s_waitcnt lgkmcnt(0)\n" \
  "s_barrier\n" \
  "s_cbranch_scc1 NOXN_%=\n" \
  "v_lshl_add_u32 v26, s61, 6, v0\n" \
  "s_lshl_b32 s62, s79, 10\n" \
  "s_add_u32 s62, s62, 81920\n" \
  "v_lshl_add_u32 v27, v26, 2, s62\n" \
  "ds_read_b32 v28, v27\n" \
  "v_cmp_le_u32 vcc, 80, v26\n" \
  "s_mul_i32 s62, s79, 80\n" \
  "s_add_u32 s62, s62, 25472\n" \
  "v_add_u32 v27, 240, v26\n" \
  "v_cndmask_b32 v27, v26, v27, vcc\n" \
  "v_add_lshl_u32 v30, v27, s62, 4\n" \
  "v_cmp_gt_u32 vcc, 160, v26\n" \
  "v_mov_b32 v27, s71\n" \
  "v_mov_b32 v29, s71\n" \
  "s_waitcnt lgkmcnt(0)\n" \
  "v_lshlrev_b32 v26, 16, v28\n" \
  "v_and_b32 v28, 0xffff0000, v28\n" \
  "s_mov_b64 exec, vcc\n" \
  "buffer_store_dwordx4 v[26:29], v30, s[40:43], 0 offen sc1\n" \
  "s_mov_b64 exec, -1\n" \
  "s_nop 0\n" \
  "NOXN_%=:\n" \
  "ds_read2st64_b64 v[8:11], v35 offset0:0 offset1:1\n" \
  "ds_read2st64_b64 v[12:15], v35 offset0:2 offset1:3\n" \
  "ds_read2st64_b64 v[16:19], v35 offset0:4 offset1:5\n" \
  "ds_read2st64_b64 v[20:23], v35 offset0:6 offset1:7\n" \
  "s_add_u32 m0, s68, 114688\n" \
  "s_nop 0\n" \
  "buffer_load_dwordx4 v136, s[48:51], s70 offen offset:0 lds\n" \
  "buffer_load_dwordx4 v137, s[48:51], s70 offen offset:1024 lds\n" \
  "buffer_load_dwordx4 v138, s[48:51], s70 offen offset:2048 lds\n" \
  "buffer_load_dwordx4 v139, s[48:51], s70 offen offset:3072 lds\n" \
  "s_add_u32 s70, s70, 128\n" \
  "s_and_b32 s70, s70, 2047\n" \
  "s_waitcnt lgkmcnt(0)\n" \
  "v_pk_add_f32 v[8:9], v[8:9], v[12:13]\n" \
  "v_pk_add_f32 v[10:11], v[10:11], v[14:15]\n" \
  "v_pk_add_f32 v[8:9], v[8:9], v[16:17]\n" \
  "v_pk_add_f32 v[10:11], v[10:11], v[18:19]\n" \
  "s_add_u32 m0, s68, 131072\n" \
  "v_pk_add_f32 v[8:9], v[8:9], v[20:21]\n" \
  "v_pk_add_f32 v[10:11], v[10:11], v[22:23]\n" \
  "v_cvt_pk_bf16_f32 v9, v8, v9\n" \
  "v_cvt_pk_bf16_f32 v11, v10, v11\n" \
  "buffer_load_dwordx4 v136, s[48:51], s70 offen offset:0 lds\n" \
  "v_lshlrev_b32 v8, 16, v9\n" \
  "v_and_b32 v9, 0xffff0000, v9\n" \
  "v_mul_f32 v12, 0xbfb8aa3b, v8\n" \
  "v_mul_f32 v13, 0xbfb8aa3b, v9\n" \
  "buffer_load_dwordx4 v137, s[48:51], s70 offen offset:1024 lds\n" \
  "v_exp_f32 v12, v12\n" \
  "v_exp_f32 v13, v13\n" \
  "v_lshlrev_b32 v10, 16, v11\n" \
  "buffer_load_dwordx4 v138, s[48:51], s70 offen offset:2048 lds\n" \
  "v_add_f32 v12, 1.0, v12\n" \
  "v_add_f32 v13, 1.0, v13\n" \
  "v_rcp_f32 v12, v12\n" \
  "v_rcp_f32 v13, v13\n" \
  "buffer_load_dwordx4 v139, s[48:51], s70 offen offset:3072 lds\n" \
  "v_and_b32 v11, 0xffff0000, v11\n" \
  "v_pk_mul_f32 v[8:9], v[8:9], v[12:13]\n" \
  "v_cvt_pk_bf16_f32 v9, v8, v9\n" \
  "v_lshlrev_b32 v8, 16, v9\n" \
  "s_add_u32 s70, s70, 128\n" \
  "v_and_b32 v9, 0xffff0000, v9\n" \
  "v_pk_mul_f32 v[8:9], v[8:9], v[10:11]\n" \
  "v_cvt_pk_bf16_f32 v8, v8, v9\n" \
  "s_and_b32 s70, s70, 2047\n" \
  "ds_write_b16 v36, v8\n" \
  "ds_write_b16_d16_hi v36, v8 offset:128\n" \
  "ds_read_b128 v[100:103], v37 offset:0\n" \
  "ds_read_b128 v[104:107], v37 offset:64\n" \
  "ds_read_b128 v[108:111], v38 offset:0\n" \
  "ds_read_b128 v[112:115], v38 offset:64\n" \
  "s_waitcnt vmcnt(8)\n" \
  "s_barrier\n" \
  "ds_read_b64_tr_b16 v[64:65], v39 offset:0\n" \
  "ds_read_b64_tr_b16 v[66:67], v39 offset:512\n" \
  "ds_read_b64_tr_b16 v[68:69], v39 offset:40960\n" \
  "s_add_u32 m0, s68, 147456\n" \
  "ds_read_b64_tr_b16 v[70:71], v39 offset:41472\n" \
  "ds_read_b64_tr_b16 v[72:73], v39 offset:2048\n" \
  "ds_read_b64_tr_b16 v[74:75], v39 offset:2560\n" \
  "buffer_load_dwordx4 v136, s[48:51], s70 offen offset:0 lds\n" \
  "ds_read_b64_tr_b16 v[76:77], v39 offset:43008\n" \
  "ds_read_b64_tr_b16 v[78:79], v39 offset:43520\n" \
  "ds_read_b64_tr_b16 v[80:81], v39 offset:4096\n" \
  "buffer_load_dwordx4 v137, s[48:51], s70 offen offset:1024 lds\n" \
  "s_waitcnt lgkmcnt(11)\n" \
  "ds_read_b64_tr_b16 v[82:83], v39 offset:4608\n" \
  "ds_read_b64_tr_b16 v[84:85], v39 offset:45056\n" \
  "ds_read_b64_tr_b16 v[86:87], v39 offset:45568\n" \
  "buffer_load_dwordx4 v138, s[48:51], s70 offen offset:2048 lds\n" \
  "s_waitcnt lgkmcnt(8)\n" \
  "v_mfma_f32_16x16x32_bf16 v[224:227], v[100:103], v[64:67], 0\n" \
  "v_mfma_f32_16x16x32_bf16 v[224:227], v[104:107], v[68:71], v[224:227]\n" \
  "buffer_load_dwordx4 v139, s[48:51], s70 offen offset:3072 lds\n" \
  "ds_read_b64_tr_b16 v[88:89], v39 offset:6144\n" \
  "ds_read_b64_tr_b16 v[90:91], v39 offset:6656\n" \
  "ds_read_b64_tr_b16 v[92:93], v39 offset:47104\n" \
  "s_add_u32 s70, s70, 128\n" \
  "ds_read_b64_tr_b16 v[94:95], v39 offset:47616\n" \
  "s_waitcnt lgkmcnt(8)\n" \
  "v_mfma_f32_16x16x32_bf16 v[224:227], v[108:111], v[72:75], v[224:227]\n" \
  "s_and_b32 s70, s70, 2047\n" \
  "v_mfma_f32_16x16x32_bf16 v[224:227], v[112:115], v[76:79], v[224:227]\n" \
  "ds_read_b64_tr_b16 v[64:65], v39 offset:8192\n" \
  "ds_read_b64_tr_b16 v[66:67], v39 offset:8704\n" \
  "s_waitcnt lgkmcnt(0)\n" \
  "ds_read_b64_tr_b16 v[68:69], v39 offset:49152\n" \
  "ds_read_b64_tr_b16 v[70:71], v39 offset:49664\n" \
  "s_nop 0\n" \
  "ds_read_b128 a[0:3], v140 offset:0\n" \
  "v_mfma_f32_16x16x32_bf16 v[228:231], v[100:103], v[80:83], 0\n" \
  "v_mfma_f32_16x16x32_bf16 v[228:231], v[104:107], v[84:87], v[228:231]\n" \
  "ds_read_b64_tr_b16 v[72:73], v39 offset:10240\n" \
  "ds_read_b128 a[4:7], v141 offset:0\n" \
  "ds_read_b64_tr_b16 v[74:75], v39 offset:10752\n" \
  "ds_read_b64_tr_b16 v[76:77], v39 offset:51200\n" \
  "ds_read_b64_tr_b16 v[78:79], v39 offset:51712\n" \
  "ds_read_b128 a[8:11], v142 offset:0\n" \
  "s_nop 0\n" \
  "v_mfma_f32_16x16x32_bf16 v[228:231], v[108:111], v[88:91], v[228:231]\n" \
  "ds_read_b128 a[12:15], v143 offset:0\n" \
  "v_mfma_f32_16x16x32_bf16 v[228:231], v[112:115], v[92:95], v[228:231]\n" \
  "ds_read_b64_tr_b16 v[80:81], v39 offset:12288\n" \
  "ds_read_b64_tr_b16 v[82:83], v39 offset:12800\n" \
  "s_waitcnt vmcnt(8) lgkmcnt(0)\n" \
  "ds_read_b64_tr_b16 v[84:85], v39 offset:53248\n" \
  "ds_read_b64_tr_b16 v[86:87], v39 offset:53760\n" \
  "s_nop 0\n" \
  "ds_read_b128 a[16:19], v140 offset:16384\n" \
  "v_mfma_f32_16x16x32_bf16 v[232:235], v[100:103], v[64:67], 0\n" \
  "v_mfma_f32_16x16x32_bf16 v[232:235], v[104:107], v[68:71], v[232:235]\n" \
  "ds_read_b64_tr_b16 v[88:89], v39 offset:14336\n" \
  "ds_read_b128 a[20:23], v141 offset:16384\n" \
  "ds_read_b64_tr_b16 v[90:91], v39 offset:14848\n" \
  "ds_read_b64_tr_b16 v[92:93], v39 offset:55296\n" \
  "ds_read_b64_tr_b16 v[94:95], v39 offset:55808\n" \
  "ds_read_b128 a[24:27], v142 offset:16384\n" \
  "s_nop 0\n" \
  "v_mfma_f32_16x16x32_bf16 v[232:235], v[108:111], v[72:75], v[232:235]\n" \
  "v_mfma_f32_16x16x32_bf16 v[232:235], v[112:115], v[76:79], v[232:235]\n" \
  "ds_read_b128 a[28:31], v143 offset:16384\n" \
  "ds_read_b64_tr_b16 v[64:65], v39 offset:16384\n" \
  "ds_read_b64_tr_b16 v[66:67], v39 offset:16896\n" \
  "ds_read_b64_tr_b16 v[68:69], v39 offset:57344\n" \
  "s_add_u32 m0, s68, 98304\n" \
  "ds_read_b64_tr_b16 v[70:71], v39 offset:57856\n" \
  "s_waitcnt lgkmcnt(12)\n" \
  "v_mfma_f32_16x16x32_bf16 v[236:239], v[100:103], v[80:83], 0\n" \
  "buffer_load_dwordx4 v136, s[48:51], s70 offen offset:0 lds\n" \
  "v_mfma_f32_16x16x32_bf16 v[236:239], v[104:107], v[84:87], v[236:239]\n" \
  "ds_read_b64_tr_b16 v[72:73], v39 offset:18432\n" \
  "ds_read_b64_tr_b16 v[74:75], v39 offset:18944\n" \
  "buffer_load_dwordx4 v137, s[48:51], s70 offen offset:1024 lds\n" \
  "ds_read_b64_tr_b16 v[76:77], v39 offset:59392\n" \
  "s_waitcnt lgkmcnt(11)\n" \
  "ds_read_b64_tr_b16 v[78:79], v39 offset:59904\n" \
  "s_waitcnt lgkmcnt(10)\n" \
  "buffer_load_dwordx4 v138, s[48:51], s70 offen offset:2048 lds\n" \
  "v_mfma_f32_16x16x32_bf16 v[236:239], v[108:111], v[88:91], v[236:239]\n" \
  "v_mfma_f32_16x16x32_bf16 v[236:239], v[112:115], v[92:95], v[236:239]\n" \
  "s_waitcnt lgkmcnt(4)\n" \
  "buffer_load_dwordx4 v139, s[48:51], s70 offen offset:3072 lds\n" \
  "v_mfma_f32_16x16x32_bf16 v[240:243], v[100:103], v[64:67], 0\n" \
  "v_mfma_f32_16x16x32_bf16 v[240:243], v[104:107], v[68:71], v[240:243]\n" \
  "s_waitcnt lgkmcnt(0)\n" \
  "s_add_u32 s70, s70, 128\n" \
  "v_mfma_f32_16x16x32_bf16 v[240:243], v[108:111], v[72:75], v[240:243]\n" \
  "v_mfma_f32_16x16x32_bf16 v[240:243], v[112:115], v[76:79], v[240:243]\n" \
  "s_and_b32 s70, s70, 2047\n" \
  "s_waitcnt vmcnt(8) lgkmcnt(0)\n" \
  "ds_read_b128 a[32:35], v140 offset:32768\n" \
  "ds_read_b128 a[36:39], v141 offset:32768\n" \
  "ds_read_b128 a[40:43], v142 offset:32768\n" \
  "ds_read_b128 a[44:47], v143 offset:32768\n" \
  "s_add_u32 m0, s68, 114688\n" \
  "s_nop 0\n" \
  "buffer_load_dwordx4 v136, s[48:51], s70 offen offset:0 lds\n" \
  "buffer_load_dwordx4 v137, s[48:51], s70 offen offset:1024 lds\n" \
  "buffer_load_dwordx4 v138, s[48:51], s70 offen offset:2048 lds\n" \
  "buffer_load_dwordx4 v139, s[48:51], s70 offen offset:3072 lds\n" \
  "s_add_u32 s70, s70, 128\n" \
  "s_and_b32 s70, s70, 2047\n" \
  "s_nop 3\n" \
  "v_mov_b32 v225, s71\n" \
  "v_mov_b32 v229, s71\n" \
  "v_mov_b32 v233, s71\n" \
  "v_mov_b32 v237, s71\n" \
  "v_mov_b32 v241, s71\n" \
  "v_mov_b32 v247, v40\n" \
  "s_movk_i32 s61, 4096\n" \
  "buffer_store_dwordx2 v[224:225], v247, s[40:43], 0 offen offset:0 sc1\n" \
  "buffer_store_dwordx2 v[228:229], v247, s[40:43], 0 offen offset:1024 sc1\n" \
  "buffer_store_dwordx2 v[232:233], v247, s[40:43], 0 offen offset:2048 sc1\n" \
  "buffer_store_dwordx2 v[236:237], v247, s[40:43], 0 offen offset:3072 sc1\n" \
  "buffer_store_dwordx2 v[240:241], v247, s[40:43], s61 offen offset:0 sc1\n" \
  "s_waitcnt vmcnt(13) lgkmcnt(0)\n" \
  "ds_read_b128 a[48:51], v140 offset:49152\n" \
  "ds_read_b128 a[52:55], v141 offset:49152\n" \
  "ds_read_b128 a[56:59], v142 offset:49152\n" \
  "ds_read_b128 a[60:63], v143 offset:49152\n" \
  "s_add_u32 m0, s68, 131072\n" \
  "s_nop 0\n" \
  "buffer_load_dwordx4 v136, s[48:51], s70 offen offset:0 lds\n" \
  "buffer_load_dwordx4 v137, s[48:51], s70 offen offset:1024 lds\n" \
  "buffer_load_dwordx4 v138, s[48:51], s70 offen offset:2048 lds\n" \
  "buffer_load_dwordx4 v139, s[48:51], s70 offen offset:3072 lds\n" \
  "s_add_u32 s70, s70, 128\n" \
  "s_and_b32 s70, s70, 2047\n" \
  "s_waitcnt vmcnt(13) lgkmcnt(0)\n" \
  "ds_read_b128 a[64:67], v140 offset:0\n" \
  "ds_read_b128 a[68:71], v141 offset:0\n" \
  "ds_read_b128 a[72:75], v142 offset:0\n" \
  "ds_read_b128 a[76:79], v143 offset:0\n" \
  "s_add_u32 m0, s68, 147456\n" \
  "s_nop 0\n" \
  "buffer_load_dwordx4 v136, s[48:51], s70 offen offset:0 lds\n" \
  "buffer_load_dwordx4 v137, s[48:51], s70 offen offset:1024 lds\n" \
  "buffer_load_dwordx4 v138, s[48:51], s70 offen offset:2048 lds\n" \
  "buffer_load_dwordx4 v139, s[48:51], s70 offen offset:3072 lds\n" \
  "s_add_u32 s70, s70, 128\n" \
  "s_and_b32 s70, s70, 2047\n" \
  "s_branch PBEG_%=\n" \
  "RGS_%=:\n" \
  "s_mov_b32 s74, 0\n" \
  "RGL_%=:\n" \
  "s_add_u32 s74, s74, 1\n" \
  "buffer_load_dwordx4 v[16:19], v3, s[40:43], 0 offen offset:0 sc1\n" \
  "buffer_load_dwordx4 v[20:23], v3, s[40:43], 0 offen offset:1024 sc1\n" \
  "s_waitcnt vmcnt(0)\n" \
  "v_xor_b32 v26, s71, v17\n" \
  "v_bitop3_b32 v26, v19, v26, s71 bitop3:0xde\n" \
  "v_bitop3_b32 v26, v21, v26, s71 bitop3:0xde\n" \
  "v_bitop3_b32 v26, v23, v26, s71 bitop3:0xde\n" \
  "v_cmp_ne_u32 vcc, 0, v26\n" \
  "s_cbranch_vccz RGK_%=\n" \
  "s_cmp_lt_u32 s74, 32768\n" \
  "s_cbranch_scc1 RGL_%=\n" \
  "s_branch RGK_%=\n" \
  "SGS_%=:\n" \
  "s_mov_b32 s74, 0\n" \
  "SGL_%=:\n" \
  "s_add_u32 s74, s74, 1\n" \
  "buffer_load_dwordx2 v[48:49], v42, s[40:43], 0 offen sc1\n" \
  "s_waitcnt vmcnt(0)\n" \
  "v_cmp_ne_u32 vcc, s71, v49\n" \
  "s_cbranch_vccz SGK_%=\n" \
  "s_cmp_lt_u32 s74, 32768\n" \
  "s_cbranch_scc1 SGL_%=\n" \
  "s_branch SGK_%=\n" \
  "GSPIN_%=:\n" \
  "s_mov_b32 s74, 0\n" \
  "GSP1_%=:\n" \
  "buffer_load_dwordx2 v[160:161], v148, s[40:43], 0 offen sc1\n" \
  "buffer_load_dwordx2 v[162:163], v149, s[40:43], 0 offen sc1\n" \
  "buffer_load_dwordx2 v[164:165], v150, s[40:43], 0 offen sc1\n" \
  "s_waitcnt vmcnt(0)\n" \
  "v_xor_b32 v56, s71, v161\n" \
  "v_bitop3_b32 v56, v163, v56, s71 bitop3:0xde\n" \
  "v_bitop3_b32 v56, v165, v56, s71 bitop3:0xde\n" \
  "v_cmp_ne_u32 vcc, 0, v56\n" \
  "s_add_u32 s74, s74, 1\n" \
  "s_cbranch_vccz GOK_%=\n" \
  "s_cmp_lt_u32 s74, 32768\n" \
  "s_cbranch_scc1 GSP1_%=\n" \
  "s_branch GOK_%=\n"
#define CL_K "v0","v1","v2","v3","v4","v5","v6","v7","v8","v9","v10","v11","v12","v13","v14","v15","v16","v17","v18","v19","v20","v21","v22","v23","v24","v25","v26","v27","v28","v29","v30","v31","v32","v33","v34","v35","v36","v37","v38","v39","v40","v41","v42","v43","v44","v45","v46","v47","v48","v49","v50","v51","v52","v53","v54","v55","v56","v57","v58","v59","v60","v61","v62","v63","v64","v65","v66","v67","v68","v69","v70","v71","v72","v73","v74","v75","v76","v77","v78","v79","v80","v81","v82","v83","v84","v85","v86","v87","v88","v89","v90","v91","v92","v93","v94","v95","v96","v97","v98","v99","v100","v101","v102","v103","v104","v105","v106","v107","v108","v109","v110","v111","v112","v113","v114","v115","v116","v117","v118","v119","v120","v121","v122","v123","v124","v125","v126","v127","v128","v129","v130","v131","v132","v133","v134","v135","v136","v137","v138","v139","v140","v141","v142","v143","v144","v145","v146","v147","v148","v149","v150","v151","v152","v153","v154","v155","v156","v157","v158","v159","v160","v161","v162","v163","v164","v165","v166","v167","v168","v169","v170","v171","v172","v173","v174","v175","v176","v177","v178","v179","v180","v181","v182","v183","v184","v185","v186","v187","v188","v189","v190","v191","v192","v193","v194","v195","v196","v197","v198","v199","v200","v201","v202","v203","v204","v205","v206","v207","v208","v209","v210","v211","v212","v213","v214","v215","v216","v217","v218","v219","v220","v221","v222","v223","v224","v225","v226","v227","v228","v229","v230","v231","v232","v233","v234","v235","v236","v237","v238","v239","v240","v241","v242","v243","v244","v245","v246","v247","v248","v250","v251","a0","a1","a2","a3","a4","a5","a6","a7","a8","a9","a10","a11","a12","a13","a14","a15","a16","a17","a18","a19","a20","a21","a22","a23","a24","a25","a26","a27","a28","a29","a30","a31","a32","a33","a34","a35","a36","a37","a38","a39","a40","a41","a42","a43","a44","a45","a46","a47","a48","a49","a50","a51","a52","a53","a54","a55","a56","a57","a58","a59","a60","a61","a62","a63","a64","a65","a66","a67","a68","a69","a70","a71","a72","a73","a74","a75","a76","a77","a78","a79","a80","a81","a82","a83","a84","a85","a86","a87","a88","a89","a90","a91","a92","a93","a94","a95","a96","a97","a98","a99","a100","a101","a102","a103","a104","a105","a106","a107","a108","a109","a110","a111","a112","a113","a114","a115","a116","a117","a118","a119","a120","a121","a122","a123","a124","a125","a126","a127","a128","a129","a130","a131","a132","a133","a134","a135","a136","a137","a138","a139","a140","a141","a142","a143","a144","a145","a146","a147","a148","a149","a150","a151","a152","a153","a154","a155","a156","a157","a158","a159","a160","a161","a162","a163","a164","a165","a166","a167","a168","a169","a170","a171","a172","a173","a174","a175","a176","a177","a178","a179","a180","a181","a182","a183","a184","a185","a186","a187","a188","a189","a190","a191","a192","a193","a194","a195","a196","a197","a198","a199","a200","a201","a202","a203","a204","a205","a206","a207","a208","a209","a210","a211","a212","a213","a214","a215","a216","a217","a218","a219","a220","a221","a222","a223","a224","a225","a226","a227","a228","a229","a230","a231","a232","a233","a234","a235","a236","a237","a238","a239","a240","a241","a242","a243","a244","a245","a246","a247","a248","a249","a250","a251","a252","a253","a254","a255","s4","s5","s6","s7","s8","s9","s10","s11","s12","s13","s14","s15","s16","s17","s18","s19","s20","s21","s22","s23","s24","s25","s26","s27","s28","s29","s30","s31","s34","s35","s36","s37","s38","s39","s40","s41","s42","s43","s44","s45","s46","s47","s48","s49","s50","s51","s52","s53","s54","s55","s56","s57","s58","s59","s60","s61","s62","s63","s64","s65","s66","s67","s68","s69","s70","s71","s72","s73","s74","s75","s76","s77","s78","s79","s80","s81","s82","s83","s84","s85","s86","s87","s88","s89","s90","s91","s92","s93","s94","s95","s96","s97","s98","s99"
#define DBG 0


constexpr u32 OFF_DBG = 23068672, WK_SIZE = OFF_DBG + (DBG ? 524288 : 0);
struct Buf { const void* p; u32 bytes, kind; };
struct Args { Buf b[11]; };

__global__ __launch_bounds__(256, 1) __attribute__((aligned(4096))) void fused_k(Args args) {
  asm volatile(ASM_K :: [ka] "s"(__builtin_amdgcn_kernarg_segment_ptr()), [bid] "s"(blockIdx.x), [tid] "{v249}"(threadIdx.x)
               : "memory", "scc", "vcc", CL_K);
}

extern "C" void kernel_launch(void* const* d_in, const int* in_sizes, int n_in, void* d_out, int out_size, void* d_ws, size_t ws_size,
                              hipStream_t stream) {
  (void)hipFuncSetAttribute((const void*)fused_k, hipFuncAttributeMaxDynamicSharedMemorySize, 163840);
  const u32 K = 0x00020000;
  Args a = {{{d_in[0], 2621440, K}, {d_ws, WK_SIZE, K}, {d_in[3], 819200, K}, {d_in[4], 819200, K}, {d_in[1], 8388608, K}, {d_in[2], 8388608, K},
             {d_in[7], 2211840, K}, {d_in[8], 2211840, K}, {d_in[6], 1280, K}, {d_in[9], 2211840, K}, {d_out, 2621440, K}}};
  fused_k<<<256, 256, 163840, stream>>>(a);
}
```
